```python
import torch

def _softmax(x, axis=-1):
    return torch.softmax(x, dim=axis)
def _gelu(x):
    return torch.nn.functional.gelu(x, approximate="tanh")
def _silu(x):
    return torch.nn.functional.silu(x)
def _sigmoid(x):
    return torch.sigmoid(x)
def _layernorm(x, axis=-1, eps=1e-5):
    m = x.mean(dim=axis, keepdim=True)
    v = x.var(dim=axis, keepdim=True, unbiased=False)
    return (x - m) / torch.sqrt(v + eps)
def _scatter_add(tgt, idx, src, axis=0):
    return tgt.clone().index_add_(axis, idx, src)
def _segment_sum(src, seg, n):
    out = torch.zeros((n,) + src.shape[1:], dtype=src.dtype)
    return out.index_add_(0, seg, src)
def _pad(x, pw):
    flat = tuple(v for pair in reversed(pw) for v in pair)
    return torch.nn.functional.pad(x, flat)

kernel_name = 'standalone__kv_cache_quant_int4_int2_kv_with_per_channel_or_per_token_scales__decode'

def setup_inputs():

    _w = (lambda a: (lambda t: t.to(torch.bfloat16) if t.is_floating_point() else t)(torch.as_tensor(a)))
    B = 8
    Sq = 1
    Skv = 4096
    Hq = 16
    Hkv = 8
    Dh = 256
    bits = 3
    G = 128
    if G <= 0 or G >= Skv:
        G = max(1, Skv // 2)
    Skv = (Skv // G) * G
    ngk = Skv // G
    qmax = (1 << bits) - 1
    return {
        "q": _w((lambda *s: torch.randn(*s, dtype=torch.bfloat16) * 3.2)(B, Hq, Sq, Dh)),
        "qk": _w(torch.randint(0, qmax + 1, (B, Hkv, Skv, Dh)).to(torch.int32)),
        "qv": _w(torch.randint(0, qmax + 1, (B, Hkv, Skv, Dh)).to(torch.int32)),
        "k_scales": _w(torch.abs((lambda *s: torch.randn(*s, dtype=torch.bfloat16) * 3.2)(B, Hkv, ngk, Dh)) * 0.05 + 0.02),
        "k_zeros": _w((lambda *s: torch.randn(*s, dtype=torch.bfloat16) * 3.2)(B, Hkv, ngk, Dh) + float(qmax) / 2.0),
        "v_scales": _w(torch.abs((lambda *s: torch.randn(*s, dtype=torch.bfloat16) * 0.05)(B, Hkv, Skv, 1)) * 0.05 + 0.02),
        "v_zeros": _w((lambda *s: torch.randn(*s, dtype=torch.bfloat16) * 0.05)(B, Hkv, Skv, 1) + float(qmax) / 2.0),
    }

def reference(q, qk, qv, k_scales, k_zeros, v_scales, v_zeros):
    B, Hq, Sq, Dh = q.shape
    Hkv, Skv = qk.shape[1], qk.shape[2]
    ngk = k_scales.shape[2]
    G = Skv // ngk
    ks = (lambda x, n, axis: torch.repeat_interleave(x, n, dim=axis))(k_scales, G, axis=2)
    kz = (lambda x, n, axis: torch.repeat_interleave(x, n, dim=axis))(k_zeros, G, axis=2)
    k = ((lambda x, dt: x.to(dt))(qk, q.dtype) - kz) * ks
    v = ((lambda x, dt: x.to(dt))(qv, q.dtype) - v_zeros) * v_scales
    rep = Hq // Hkv
    if rep > 1:
        k = (lambda x, n, axis: torch.repeat_interleave(x, n, dim=axis))(k, rep, axis=1)
        v = (lambda x, n, axis: torch.repeat_interleave(x, n, dim=axis))(v, rep, axis=1)
    scale = float(Dh) ** -0.5
    logits = torch.einsum("bhqd,bhkd->bhqk", q, k) * scale
    p = _softmax(logits, axis=-1)
    o = torch.einsum("bhqk,bhkd->bhqd", p, v)
    return o
```

```cpp
#include <hip/hip_runtime.h>
#include <hip/hip_bf16.h>
#include <stdint.h>

namespace {

constexpr int B = 8, HQ = 16, HKV = 8, SKV = 4096, DH = 256, GRP = 128, NGK = SKV / GRP, REP = HQ / HKV;
static_assert(REP == 2 && DH == 256, "layout assumes 2 q heads per kv head and 256 dims (one 1 KiB row per wave load)");

#define NSPLIT 8
#define WAVES 4
constexpr int NTHR = WAVES * 64;
constexpr int TOK_PER_WG = SKV / NSPLIT;
constexpr int BLK = 32;
constexpr int NIT = TOK_PER_WG / (BLK * WAVES);
static_assert(TOK_PER_WG % (BLK * WAVES) == 0, "even split");

typedef int i32x4 __attribute__((ext_vector_type(4)));
typedef float f32x4 __attribute__((ext_vector_type(4)));
typedef unsigned u32x2 __attribute__((ext_vector_type(2)));

__device__ __forceinline__ float bf_lo(unsigned u) { return __builtin_bit_cast(float, u << 16); }
__device__ __forceinline__ float bf_hi(unsigned u) { return __builtin_bit_cast(float, u & 0xffff0000u); }

template <int CTRL> __device__ __forceinline__ float dpp(float x) {
  return __builtin_bit_cast(float, __builtin_amdgcn_mov_dpp(__builtin_bit_cast(int, x), CTRL, 0xf, 0xf, true));
}
constexpr int XOR1 = 0xB1, XOR2 = 0x4E, XOR7 = 0x141, XOR8 = 0x128;

__device__ __forceinline__ float tr32(float a, float b) {
  auto r = __builtin_amdgcn_permlane32_swap(__float_as_uint(a), __float_as_uint(b), false, false);
  return __uint_as_float(r[0]) + __uint_as_float(r[1]);
}
__device__ __forceinline__ float tr16(float a, float b) {
  auto r = __builtin_amdgcn_permlane16_swap(__float_as_uint(a), __float_as_uint(b), false, false);
  return __uint_as_float(r[0]) + __uint_as_float(r[1]);
}
template <int CTRL> __device__ __forceinline__ float trrow(float lo, float hi, bool bit) {
  const float keep = bit ? hi : lo, send = bit ? lo : hi;
  return keep + dpp<CTRL>(send);
}
__device__ __forceinline__ float half_max(float x) {
  x = fmaxf(x, dpp<XOR1>(x)); x = fmaxf(x, dpp<XOR2>(x)); x = fmaxf(x, dpp<XOR7>(x)); x = fmaxf(x, dpp<XOR8>(x));
  auto r = __builtin_amdgcn_permlane16_swap(__float_as_uint(x), __float_as_uint(x), false, false);
  return fmaxf(__uint_as_float(r[0]), __uint_as_float(r[1]));
}
__device__ __forceinline__ float half_sum(float x) {
  x += dpp<XOR1>(x); x += dpp<XOR2>(x); x += dpp<XOR7>(x); x += dpp<XOR8>(x);
  auto r = __builtin_amdgcn_permlane16_swap(__float_as_uint(x), __float_as_uint(x), false, false);
  return __uint_as_float(r[0]) + __uint_as_float(r[1]);
}
__device__ __forceinline__ float rdlane(float x, int l) {
  return __builtin_bit_cast(float, __builtin_amdgcn_readlane(__builtin_bit_cast(int, x), l));
}

constexpr int PART = 2 * DH + 4;

__global__ void __launch_bounds__(NTHR) attn_split(const unsigned short* __restrict__ q, const int* __restrict__ qk,
                                                   const int* __restrict__ qv, const unsigned short* __restrict__ ks,
                                                   const unsigned short* __restrict__ kz, const unsigned short* __restrict__ vs,
                                                   const unsigned short* __restrict__ vz, float* __restrict__ part) {
  __shared__ __attribute__((aligned(16))) float lds[WAVES * PART];
  const int pair = blockIdx.x / NSPLIT, split = blockIdx.x % NSPLIT;
  const int tid = threadIdx.x, lane = tid & 63, wave = __builtin_amdgcn_readfirstlane(tid >> 6);
  constexpr float C2 = 0.0625f * 1.4426950408889634f;

  float qf[2][4];
#pragma unroll
  for (int h = 0; h < 2; ++h) {
    const u32x2 u = *reinterpret_cast<const u32x2*>(q + ((size_t)pair * 2 + h) * DH + 4 * lane);
    qf[h][0] = bf_lo(u[0]) * C2; qf[h][1] = bf_hi(u[0]) * C2; qf[h][2] = bf_lo(u[1]) * C2; qf[h][3] = bf_hi(u[1]) * C2;
  }
  const int* kbase = qk + (size_t)pair * SKV * DH + 4 * lane;
  const int* vbase = qv + (size_t)pair * SKV * DH + 4 * lane;
  const int mytok = ((lane >> 2) & 3) * 8 + ((lane >> 4) & 1) * 4 + (lane & 3);

  float m = -INFINITY, l = 0.f, zs = 0.f;
  float acc[2][4] = {{0.f, 0.f, 0.f, 0.f}, {0.f, 0.f, 0.f, 0.f}};

  for (int it = 0; it < NIT; ++it) {
    const int t0 = split * TOK_PER_WG + (it * WAVES + wave) * BLK;
    const int g = t0 / GRP;
    const u32x2 us = *reinterpret_cast<const u32x2*>(ks + ((size_t)pair * NGK + g) * DH + 4 * lane);
    const u32x2 uz = *reinterpret_cast<const u32x2*>(kz + ((size_t)pair * NGK + g) * DH + 4 * lane);
    const float vsc = bf_lo(vs[(size_t)pair * SKV + t0 + mytok]);
    const float vzr = bf_lo(vz[(size_t)pair * SKV + t0 + mytok]);
    float a[2][4], c[2];
    {
      const float s4[4] = {bf_lo(us[0]), bf_hi(us[0]), bf_lo(us[1]), bf_hi(us[1])};
      const float z4[4] = {bf_lo(uz[0]), bf_hi(uz[0]), bf_lo(uz[1]), bf_hi(uz[1])};
#pragma unroll
      for (int h = 0; h < 2; ++h) {
        float cc = 0.f;
#pragma unroll
        for (int e = 0; e < 4; ++e) { a[h][e] = qf[h][e] * s4[e]; cc = fmaf(-a[h][e], z4[e], cc); }
        c[h] = cc;
      }
    }
    float y[16];
#pragma unroll
    for (int s = 0; s < 4; ++s) {
      i32x4 kk[8];
#pragma unroll
      for (int t = 0; t < 8; ++t) kk[t] = __builtin_nontemporal_load(reinterpret_cast<const i32x4*>(kbase + (size_t)(t0 + s * 8 + t) * DH));
      float x[2][8];
#pragma unroll
      for (int t = 0; t < 8; ++t) {
        float x0 = c[0], x1 = c[1];
#pragma unroll
        for (int e = 0; e < 4; ++e) { const float f = (float)kk[t][e]; x0 = fmaf(a[0][e], f, x0); x1 = fmaf(a[1][e], f, x1); }
        x[0][t] = x0; x[1][t] = x1;
      }
      float u[8];
#pragma unroll
      for (int t = 0; t < 8; ++t) u[t] = tr32(x[0][t], x[1][t]);
#pragma unroll
      for (int t = 0; t < 4; ++t) y[s * 4 + t] = tr16(u[t], u[t + 4]);
    }
#pragma unroll
    for (int i = 0; i < 8; ++i) y[i] = trrow<XOR8>(y[i], y[i + 8], (lane & 8) != 0);
#pragma unroll
    for (int i = 0; i < 4; ++i) y[i] = trrow<XOR7>(y[i], y[i + 4], (lane & 4) != 0);
#pragma unroll
    for (int i = 0; i < 2; ++i) y[i] = trrow<XOR2>(y[i], y[i + 2], (lane & 2) != 0);
    const float sc = trrow<XOR1>(y[0], y[1], (lane & 1) != 0);

    const float mn = fmaxf(m, half_max(sc));
    const float alpha = __builtin_amdgcn_exp2f(m - mn);
    const float p = __builtin_amdgcn_exp2f(sc - mn);
    m = mn;
    l = fmaf(l, alpha, p);
    const float pv = p * vsc;
    zs = fmaf(zs, alpha, pv * vzr);
    const float al0 = rdlane(alpha, 0), al1 = rdlane(alpha, 32);
#pragma unroll
    for (int e = 0; e < 4; ++e) { acc[0][e] *= al0; acc[1][e] *= al1; }
#pragma unroll
    for (int s = 0; s < 4; ++s) {
      i32x4 vv[8];
#pragma unroll
      for (int t = 0; t < 8; ++t) vv[t] = __builtin_nontemporal_load(reinterpret_cast<const i32x4*>(vbase + (size_t)(t0 + s * 8 + t) * DH));
#pragma unroll
      for (int t = 0; t < 8; ++t) {
        const int ln = (t >> 2) * 16 + s * 4 + (t & 3);
        const float p0 = rdlane(pv, ln), p1 = rdlane(pv, ln + 32);
#pragma unroll
        for (int e = 0; e < 4; ++e) { const float f = (float)vv[t][e]; acc[0][e] = fmaf(p0, f, acc[0][e]); acc[1][e] = fmaf(p1, f, acc[1][e]); }
      }
    }
  }
  l = half_sum(l); zs = half_sum(zs);
  const float z0 = rdlane(zs, 0), z1 = rdlane(zs, 32);
  float* rec = lds + wave * PART;
  *reinterpret_cast<f32x4*>(rec + 4 * lane) = f32x4{acc[0][0] - z0, acc[0][1] - z0, acc[0][2] - z0, acc[0][3] - z0};
  *reinterpret_cast<f32x4*>(rec + DH + 4 * lane) = f32x4{acc[1][0] - z1, acc[1][1] - z1, acc[1][2] - z1, acc[1][3] - z1};
  if ((lane & 31) == 0) { rec[2 * DH + (lane >> 5)] = m; rec[2 * DH + 2 + (lane >> 5)] = l; }
  __syncthreads();
  float* dst = part + (size_t)blockIdx.x * PART;
#pragma unroll
  for (int i = tid; i < 2 * DH; i += NTHR) {
    const int h = i / DH;
    float M = -INFINITY;
#pragma unroll
    for (int w = 0; w < WAVES; ++w) M = fmaxf(M, lds[w * PART + 2 * DH + h]);
    float o = 0.f, L = 0.f;
#pragma unroll
    for (int w = 0; w < WAVES; ++w) {
      const float sw = __builtin_amdgcn_exp2f(lds[w * PART + 2 * DH + h] - M);
      o = fmaf(sw, lds[w * PART + i], o);
      L = fmaf(sw, lds[w * PART + 2 * DH + 2 + h], L);
    }
    dst[i] = o;
    if ((i & (DH - 1)) == 0) { dst[2 * DH + h] = M; dst[2 * DH + 2 + h] = L; }
  }
}

__global__ void __launch_bounds__(256) attn_combine(const float* __restrict__ part, __hip_bfloat16* __restrict__ out) {
  const int pair = blockIdx.x >> 1, h = blockIdx.x & 1, d = threadIdx.x;
  const float* p = part + (size_t)pair * NSPLIT * PART;
  float M = -INFINITY;
#pragma unroll
  for (int s = 0; s < NSPLIT; ++s) M = fmaxf(M, p[s * PART + 2 * DH + h]);
  float o = 0.f, L = 0.f;
#pragma unroll
  for (int s = 0; s < NSPLIT; ++s) {
    const float sw = __builtin_amdgcn_exp2f(p[s * PART + 2 * DH + h] - M);
    o = fmaf(sw, p[s * PART + h * DH + d], o);
    L = fmaf(sw, p[s * PART + 2 * DH + 2 + h], L);
  }
  out[(size_t)blockIdx.x * DH + d] = __float2bfloat16(o / L);
}

}

extern "C" void kernel_launch(void* const* d_in, const int* in_sizes, int n_in, void* d_out, int out_size, void* d_ws,
                              size_t ws_size, hipStream_t stream) {
  const unsigned short* q = (const unsigned short*)d_in[0];
  const int* qk = (const int*)d_in[1];
  const int* qv = (const int*)d_in[2];
  const unsigned short* ks = (const unsigned short*)d_in[3];
  const unsigned short* kz = (const unsigned short*)d_in[4];
  const unsigned short* vs = (const unsigned short*)d_in[5];
  const unsigned short* vz = (const unsigned short*)d_in[6];
  float* part = (float*)d_ws;
  attn_split<<<B * HKV * NSPLIT, NTHR, 0, stream>>>(q, qk, qv, ks, kz, vs, vz, part);
  attn_combine<<<B * HQ, 256, 0, stream>>>(part, (__hip_bfloat16*)d_out);
}
```

```cpp
#include <hip/hip_runtime.h>
#include <hip/hip_bf16.h>
#include <stdint.h>

namespace {

constexpr int B = 8, HQ = 16, HKV = 8, SKV = 4096, DH = 256, GRP = 128, NGK = SKV / GRP;
constexpr int NSPLIT = 4, NWG = B * HKV * NSPLIT, REG = SKV / NSPLIT;
#define WAVES 4
#define TT 4
#define NB 1
#define SKR 32
constexpr int NTHR = WAVES * 64;
constexpr int CH = WAVES * TT;
static_assert(GRP % CH == 0 && SKR % CH == 0 && SKR < GRP && (TT == 2 || TT == 4 || TT == 8), "shape");
constexpr int NGMAX = REG / GRP + (SKR ? 1 : 0);
constexpr int TOKMAX = NGMAX * GRP;

typedef int i32x4 __attribute__((ext_vector_type(4)));
typedef float f32x4 __attribute__((ext_vector_type(4)));
typedef float f32x2 __attribute__((ext_vector_type(2)));
typedef unsigned u32x2 __attribute__((ext_vector_type(2)));
typedef unsigned long long u64;

__device__ __forceinline__ float bf_lo(unsigned u) { return __builtin_bit_cast(float, u << 16); }
__device__ __forceinline__ float bf_hi(unsigned u) { return __builtin_bit_cast(float, u & 0xffff0000u); }

template <int CTRL> __device__ __forceinline__ float dpp(float x) {
  return __builtin_bit_cast(float, __builtin_amdgcn_mov_dpp(__builtin_bit_cast(int, x), CTRL, 0xf, 0xf, true));
}
constexpr int XOR1 = 0xB1, XOR2 = 0x4E, XOR7 = 0x141, XOR8 = 0x128;

__device__ __forceinline__ float tr32(float a, float b) {
  auto r = __builtin_amdgcn_permlane32_swap(__float_as_uint(a), __float_as_uint(b), false, false);
  return __uint_as_float(r[0]) + __uint_as_float(r[1]);
}
__device__ __forceinline__ float tr16(float a, float b) {
  auto r = __builtin_amdgcn_permlane16_swap(__float_as_uint(a), __float_as_uint(b), false, false);
  return __uint_as_float(r[0]) + __uint_as_float(r[1]);
}
__device__ __forceinline__ float max16(float x) {
  auto r = __builtin_amdgcn_permlane16_swap(__float_as_uint(x), __float_as_uint(x), false, false);
  return fmaxf(__uint_as_float(r[0]), __uint_as_float(r[1]));
}
template <int CTRL> __device__ __forceinline__ float trrow(float lo, float hi, bool bit) {
  return (bit ? hi : lo) + dpp<CTRL>(bit ? lo : hi);
}
__device__ __forceinline__ float rdlane(float x, int l) {
  return __builtin_bit_cast(float, __builtin_amdgcn_readlane(__builtin_bit_cast(int, x), l));
}
constexpr int TSH = TT == 8 ? 2 : TT == 4 ? 3 : 4;
__device__ __forceinline__ float reduce_scores(float (&u)[TT], int lane) {
#pragma unroll
  for (int t = 0; t < TT / 2; ++t) u[t] = tr16(u[t], u[t + TT / 2]);
  float s;
  if constexpr (TT == 8) {
    const float y0 = trrow<XOR8>(u[0], u[2], (lane & 8) != 0), y1 = trrow<XOR8>(u[1], u[3], (lane & 8) != 0);
    s = trrow<XOR7>(y0, y1, (lane & 4) != 0);
    s += dpp<XOR2>(s); s += dpp<XOR1>(s);
  } else {
    if constexpr (TT == 4) s = trrow<XOR8>(u[0], u[1], (lane & 8) != 0);
    else s = u[0] + dpp<XOR8>(u[0]);
    s += dpp<XOR1>(s); s += dpp<XOR2>(s); s += dpp<XOR7>(s);
  }
  return s;
}
__device__ __forceinline__ float tok_max(float x) {
  if constexpr (TT == 8) x = fmaxf(x, dpp<XOR7>(x));
  if constexpr (TT >= 4) x = fmaxf(x, dpp<XOR8>(x));
  return max16(x);
}
__device__ __forceinline__ float tok_sum(float x) {
  if constexpr (TT == 8) x += dpp<XOR7>(x);
  if constexpr (TT >= 4) x += dpp<XOR8>(x);
  return tr16(x, x);
}

constexpr int PART = 2 * DH + 4;
constexpr int L_A = 0;
constexpr int L_C = L_A + (NGMAX + 1) * 2 * DH;
constexpr int L_V = L_C + (NGMAX + 1) * 128;
constexpr int L_R = L_V + (TOKMAX + NB * CH) * 2;
constexpr int L_TOTAL = L_R + WAVES * PART;

#define TS 0
#define STAMP(base, k) do {} while (0)

__global__ void __launch_bounds__(NTHR) attn_split(const unsigned short* __restrict__ q, const int* __restrict__ qk,
                                                   const int* __restrict__ qv, const unsigned short* __restrict__ ks,
                                                   const unsigned short* __restrict__ kz, const unsigned short* __restrict__ vs,
                                                   const unsigned short* __restrict__ vz, float* __restrict__ part) {
  __shared__ __attribute__((aligned(16))) float lds[L_TOTAL];
  STAMP(64u << 20, 0);
  const int bid = blockIdx.x, pair = bid >> 2, odd = bid & 1;
  const int tid = threadIdx.x, lane = tid & 63, wave = __builtin_amdgcn_readfirstlane(tid >> 6);
  constexpr float C2 = 0.0625f * 1.4426950408889634f;
  const int rbase = bid * REG, r0 = odd ? SKR : 0, r1 = odd ? REG : REG + SKR;
  const int ng = odd ? REG / GRP : NGMAX;
  const __amdgpu_buffer_rsrc_t rk = __builtin_amdgcn_make_buffer_rsrc((void*)(qk + (size_t)rbase * DH), 0, r1 * (DH * 4), 0x00020000);
  const __amdgpu_buffer_rsrc_t rv = __builtin_amdgcn_make_buffer_rsrc((void*)(qv + (size_t)rbase * DH), 0, r1 * (DH * 4), 0x00020000);
  const int voff = lane * 16 + wave * 1024;

  u32x2 uq[2];
#pragma unroll
  for (int h = 0; h < 2; ++h) uq[h] = __builtin_nontemporal_load(reinterpret_cast<const u32x2*>(q + ((size_t)pair * 2 + h) * DH + 4 * lane));
  constexpr int GPW = (NGMAX + WAVES - 1) / WAVES;
  u32x2 us[GPW], uz[GPW];
#pragma unroll
  for (int i = 0; i < GPW; ++i) {
    const int g = wave + i * WAVES, gc = g < ng ? g : 0;
    const size_t o = ((size_t)(rbase / GRP) + gc) * DH + 4 * lane;
    us[i] = __builtin_nontemporal_load(reinterpret_cast<const u32x2*>(ks + o)); uz[i] = __builtin_nontemporal_load(reinterpret_cast<const u32x2*>(kz + o));
  }
  constexpr int VPT = (TOKMAX / 2 + NTHR - 1) / NTHR;
  unsigned uvs[VPT], uvz[VPT];
#pragma unroll
  for (int i = 0; i < VPT; ++i) {
    const int t2 = tid + i * NTHR, tc = 2 * t2 < r1 ? t2 : 0;
    uvs[i] = __builtin_nontemporal_load(reinterpret_cast<const unsigned*>(vs + rbase + 2 * tc)); uvz[i] = __builtin_nontemporal_load(reinterpret_cast<const unsigned*>(vz + rbase + 2 * tc));
  }

  i32x4 kk[NB][TT], vv[NB][TT];
#define LD(dst, rs, row_) do { __builtin_amdgcn_sched_barrier(0); _Pragma("unroll") for (int t_ = 0; t_ < TT; ++t_) \
    dst[t_] = __builtin_bit_cast(i32x4, __builtin_amdgcn_raw_buffer_load_b128(rs, voff, ((row_) + t_ * WAVES) * 1024, 2)); __builtin_amdgcn_sched_barrier(0); } while (0)
#pragma unroll
  for (int s = 0; s < NB; ++s) { LD(kk[s], rk, r0 + s * CH); LD(vv[s], rv, r0 + s * CH); }

  {
    float qf[2][4];
#pragma unroll
    for (int h = 0; h < 2; ++h) { qf[h][0] = bf_lo(uq[h][0]) * C2; qf[h][1] = bf_hi(uq[h][0]) * C2; qf[h][2] = bf_lo(uq[h][1]) * C2; qf[h][3] = bf_hi(uq[h][1]) * C2; }
#pragma unroll
    for (int i = 0; i < GPW; ++i) {
      const int g_ = wave + i * WAVES, g = g_ < NGMAX ? g_ : NGMAX;
      {
        const float s4[4] = {bf_lo(us[i][0]), bf_hi(us[i][0]), bf_lo(us[i][1]), bf_hi(us[i][1])};
        const float z4[4] = {bf_lo(uz[i][0]), bf_hi(uz[i][0]), bf_lo(uz[i][1]), bf_hi(uz[i][1])};
        f32x2 c;
#pragma unroll
        for (int h = 0; h < 2; ++h) {
          f32x4 a; float cc = 0.f;
#pragma unroll
          for (int e = 0; e < 4; ++e) { a[e] = qf[h][e] * s4[e]; cc = fmaf(-a[e], z4[e], cc); }
          *reinterpret_cast<f32x4*>(lds + L_A + (g * 2 + h) * DH + 4 * lane) = a;
          c[h] = cc;
        }
        *reinterpret_cast<f32x2*>(lds + L_C + g * 128 + 2 * lane) = c;
      }
    }
#pragma unroll
    for (int i = 0; i < VPT; ++i) {
      const int t2_ = tid + i * NTHR, t2 = t2_ < TOKMAX / 2 ? t2_ : TOKMAX / 2;
      {
        const float s0 = bf_lo(uvs[i]), s1 = bf_hi(uvs[i]);
        *reinterpret_cast<f32x4*>(lds + L_V + 4 * t2) = f32x4{s0, s0 * bf_lo(uvz[i]), s1, s1 * bf_hi(uvz[i])};
      }
    }
  }
  __syncthreads();
  STAMP(64u << 20, 1);

  float m = -INFINITY, l = 0.f, zs = 0.f;
  float acc[2][4] = {{0.f, 0.f, 0.f, 0.f}, {0.f, 0.f, 0.f, 0.f}};
  const int myt = (lane >> TSH) & (TT - 1);
  const int voffl = (myt * WAVES + wave) * 2;

#pragma unroll 1
  for (int r = r0; r < r1; r += NB * CH) {
#pragma unroll
    for (int s = 0; s < NB; ++s) {
      const int row = r + s * CH;
      const bool live = NB == 1 || row < r1;
      const int rowc = live ? row : r0;
      const int g = rowc >> 7;
      const f32x4 a0 = *reinterpret_cast<const f32x4*>(lds + L_A + (g * 2) * DH + 4 * lane);
      const f32x4 a1 = *reinterpret_cast<const f32x4*>(lds + L_A + (g * 2 + 1) * DH + 4 * lane);
      const f32x2 c = *reinterpret_cast<const f32x2*>(lds + L_C + g * 128 + 2 * lane);
      const f32x2 vsz = *reinterpret_cast<const f32x2*>(lds + L_V + 2 * rowc + voffl);
      float u[TT];
#pragma unroll
      for (int t = 0; t < TT; ++t) {
        float x0 = c[0], x1 = c[1];
#pragma unroll
        for (int e = 0; e < 4; ++e) { const float f = (float)kk[s][t][e]; x0 = fmaf(a0[e], f, x0); x1 = fmaf(a1[e], f, x1); }
        u[t] = tr32(x0, x1);
      }
#pragma unroll
      for (int t = 0; t < TT; ++t) asm volatile("" : "+v"(u[t]));
      LD(kk[s], rk, row + NB * CH);
      float sc = reduce_scores(u, lane);
      if (NB > 1) sc = live ? sc : -INFINITY;
      const float mn = fmaxf(m, tok_max(sc));
      const float alpha = __builtin_amdgcn_exp2f(m - mn);
      const float pr = __builtin_amdgcn_exp2f(sc - mn);
      m = mn; l = fmaf(l, alpha, pr); zs = fmaf(zs, alpha, pr * vsz[1]);
      const float pv = pr * vsz[0];
      const float al0 = rdlane(alpha, 0), al1 = rdlane(alpha, 32);
#pragma unroll
      for (int e = 0; e < 4; ++e) { acc[0][e] *= al0; acc[1][e] *= al1; }
#pragma unroll
      for (int t = 0; t < TT; ++t) {
        const float p0 = rdlane(pv, t << TSH), p1 = rdlane(pv, 32 + (t << TSH));
#pragma unroll
        for (int e = 0; e < 4; ++e) { const float f = (float)vv[s][t][e]; acc[0][e] = fmaf(p0, f, acc[0][e]); acc[1][e] = fmaf(p1, f, acc[1][e]); }
      }
      asm volatile("" : "+v"(acc[0][0]), "+v"(acc[0][1]), "+v"(acc[0][2]), "+v"(acc[0][3]), "+v"(acc[1][0]), "+v"(acc[1][1]), "+v"(acc[1][2]), "+v"(acc[1][3]));
      LD(vv[s], rv, row + NB * CH);
    }
  }
  STAMP(64u << 20, 2);

  {
    const float l_ = tok_sum(l), z_ = tok_sum(zs);
    const float z0 = rdlane(z_, 0), z1 = rdlane(z_, 32);
    float* rec = lds + L_R + wave * PART;
    *reinterpret_cast<f32x4*>(rec + 4 * lane) = f32x4{acc[0][0] - z0, acc[0][1] - z0, acc[0][2] - z0, acc[0][3] - z0};
    *reinterpret_cast<f32x4*>(rec + DH + 4 * lane) = f32x4{acc[1][0] - z1, acc[1][1] - z1, acc[1][2] - z1, acc[1][3] - z1};
    if ((lane & 31) == 0) { rec[2 * DH + (lane >> 5)] = m; rec[2 * DH + 2 + (lane >> 5)] = l_; }
  }
  __syncthreads();
  float* dst = part + (size_t)bid * PART;
#pragma unroll
  for (int i = tid; i < 2 * DH; i += NTHR) {
    const int h = i >> 8;
    const float* recs = lds + L_R;
    float mm[WAVES], M = -INFINITY;
#pragma unroll
    for (int x = 0; x < WAVES; ++x) { mm[x] = recs[x * PART + 2 * DH + h]; M = fmaxf(M, mm[x]); }
    float o = 0.f, L = 0.f;
#pragma unroll
    for (int x = 0; x < WAVES; ++x) {
      const float sw = __builtin_amdgcn_exp2f(mm[x] - M);
      o = fmaf(sw, recs[x * PART + i], o);
      L = fmaf(sw, recs[x * PART + 2 * DH + 2 + h], L);
    }
    dst[i] = o;
    if ((i & (DH - 1)) < 2) dst[2 * DH + (i & 1) * 2 + h] = (i & 1) ? L : M;
  }
  STAMP(64u << 20, 3);
}

__global__ void __launch_bounds__(256) attn_combine(const float* __restrict__ part, __hip_bfloat16* __restrict__ out) {
  STAMP(65u << 20, 0);
  const int pair = blockIdx.x >> 1, h = blockIdx.x & 1, d = threadIdx.x;
  const float* p = part + (size_t)pair * NSPLIT * PART;
  float mm[NSPLIT], ll[NSPLIT], oo[NSPLIT];
#pragma unroll
  for (int s = 0; s < NSPLIT; ++s) { mm[s] = p[s * PART + 2 * DH + h]; ll[s] = p[s * PART + 2 * DH + 2 + h]; oo[s] = p[s * PART + h * DH + d]; }
  float M = -INFINITY;
#pragma unroll
  for (int s = 0; s < NSPLIT; ++s) M = fmaxf(M, mm[s]);
  float o = 0.f, L = 0.f;
#pragma unroll
  for (int s = 0; s < NSPLIT; ++s) {
    const float sw = __builtin_amdgcn_exp2f(mm[s] - M);
    o = fmaf(sw, oo[s], o);
    L = fmaf(sw, ll[s], L);
  }
  out[(size_t)blockIdx.x * DH + d] = __float2bfloat16(o / L);
  STAMP(65u << 20, 3);
}

}

extern "C" void kernel_launch(void* const* d_in, const int* in_sizes, int n_in, void* d_out, int out_size, void* d_ws,
                              size_t ws_size, hipStream_t stream) {
  const unsigned short* q = (const unsigned short*)d_in[0];
  const int* qk = (const int*)d_in[1];
  const int* qv = (const int*)d_in[2];
  const unsigned short* ks = (const unsigned short*)d_in[3];
  const unsigned short* kz = (const unsigned short*)d_in[4];
  const unsigned short* vs = (const unsigned short*)d_in[5];
  const unsigned short* vz = (const unsigned short*)d_in[6];
  float* part = (float*)d_ws;
  attn_split<<<NWG, NTHR, 0, stream>>>(q, qk, qv, ks, kz, vs, vz, part);
  attn_combine<<<B * HQ, 256, 0, stream>>>(part, (__hip_bfloat16*)d_out);
}
```

```cpp
#include <hip/hip_runtime.h>
#include <hip/hip_bf16.h>
#include <stdint.h>

namespace {

constexpr int B = 8, HQ = 16, HKV = 8, SKV = 4096, DH = 256, GRP = 128;
constexpr int NSPLIT = 4, NWG = B * HKV * NSPLIT, REG = SKV / NSPLIT;
#define WAVES 4
#define TT 4
#define SKR 0
#define EARLY 0
constexpr int NTHR = WAVES * 64;
constexpr int CH = WAVES * TT;
static_assert(GRP % CH == 0 && SKR % (2 * CH) == 0 && SKR < GRP && (TT == 2 || TT == 4 || TT == 8), "shape");
constexpr int NGMAX = REG / GRP + (SKR ? 1 : 0);
constexpr int TOKMAX = NGMAX * GRP;
constexpr int JMAX = (REG + SKR) / WAVES;
constexpr int JPAD = (JMAX + 31) / 32 * 32 + 2 * TT;

typedef int i32x4 __attribute__((ext_vector_type(4)));
typedef float f32x4 __attribute__((ext_vector_type(4)));
typedef float f32x2 __attribute__((ext_vector_type(2)));
typedef unsigned u32x2 __attribute__((ext_vector_type(2)));
typedef __bf16 bf16x2 __attribute__((ext_vector_type(2)));

__device__ __forceinline__ float bf_lo(unsigned u) { return __builtin_bit_cast(float, u << 16); }
__device__ __forceinline__ float bf_hi(unsigned u) { return __builtin_bit_cast(float, u & 0xffff0000u); }
__device__ __forceinline__ float dot2(unsigned a, unsigned b, float c) {
  return __builtin_amdgcn_fdot2_f32_bf16(__builtin_bit_cast(bf16x2, a), __builtin_bit_cast(bf16x2, b), c, false);
}
__device__ __forceinline__ unsigned pkbf(float lo, float hi) {
  bf16x2 r; r[0] = (__bf16)lo; r[1] = (__bf16)hi;
  return __builtin_bit_cast(unsigned, r);
}

template <int CTRL> __device__ __forceinline__ float dpp(float x) {
  return __builtin_bit_cast(float, __builtin_amdgcn_mov_dpp(__builtin_bit_cast(int, x), CTRL, 0xf, 0xf, true));
}
constexpr int XOR1 = 0xB1, XOR2 = 0x4E, XOR7 = 0x141, XOR8 = 0x128;

__device__ __forceinline__ float tr32(float a, float b) {
  auto r = __builtin_amdgcn_permlane32_swap(__float_as_uint(a), __float_as_uint(b), false, false);
  return __uint_as_float(r[0]) + __uint_as_float(r[1]);
}
__device__ __forceinline__ float tr16(float a, float b) {
  auto r = __builtin_amdgcn_permlane16_swap(__float_as_uint(a), __float_as_uint(b), false, false);
  return __uint_as_float(r[0]) + __uint_as_float(r[1]);
}
__device__ __forceinline__ float max16(float x) {
  auto r = __builtin_amdgcn_permlane16_swap(__float_as_uint(x), __float_as_uint(x), false, false);
  return fmaxf(__uint_as_float(r[0]), __uint_as_float(r[1]));
}
template <int CTRL> __device__ __forceinline__ float trrow(float lo, float hi, bool bit) {
  return (bit ? hi : lo) + dpp<CTRL>(bit ? lo : hi);
}
__device__ __forceinline__ float rdlane(float x, int l) {
  return __builtin_bit_cast(float, __builtin_amdgcn_readlane(__builtin_bit_cast(int, x), l));
}
constexpr int TSH = TT == 8 ? 2 : TT == 4 ? 3 : 4;
__device__ __forceinline__ float reduce_scores(float (&u)[TT], int lane) {
#pragma unroll
  for (int t = 0; t < TT / 2; ++t) u[t] = tr16(u[t], u[t + TT / 2]);
  float s;
  if constexpr (TT == 8) {
    const float y0 = trrow<XOR8>(u[0], u[2], (lane & 8) != 0), y1 = trrow<XOR8>(u[1], u[3], (lane & 8) != 0);
    s = trrow<XOR7>(y0, y1, (lane & 4) != 0);
    s += dpp<XOR2>(s); s += dpp<XOR1>(s);
  } else {
    if constexpr (TT == 4) s = trrow<XOR8>(u[0], u[1], (lane & 8) != 0);
    else s = u[0] + dpp<XOR8>(u[0]);
    s += dpp<XOR1>(s); s += dpp<XOR2>(s); s += dpp<XOR7>(s);
  }
  return s;
}
__device__ __forceinline__ float half_max(float x) {
  x = fmaxf(x, dpp<XOR1>(x)); x = fmaxf(x, dpp<XOR2>(x)); x = fmaxf(x, dpp<XOR7>(x)); x = fmaxf(x, dpp<XOR8>(x));
  return max16(x);
}
__device__ __forceinline__ float half_sum(float x) {
  x += dpp<XOR1>(x); x += dpp<XOR2>(x); x += dpp<XOR7>(x); x += dpp<XOR8>(x);
  return tr16(x, x);
}

constexpr int PART = 2 * DH + 4;
constexpr int L_A = 0;
constexpr int L_V = L_A + (NGMAX + 1) * 2 * DH;
constexpr int L_S = L_V + (TOKMAX + 2) * 2;
constexpr int L_R = L_S + WAVES * JPAD * 2;
constexpr int L_TOTAL = L_R + WAVES * PART;

#define TS 0
#define STAMP(base, k) do {} while (0)

__global__ void __launch_bounds__(NTHR) attn_split(const unsigned short* __restrict__ q, const int* __restrict__ qk,
                                                   const int* __restrict__ qv, const unsigned short* __restrict__ ks,
                                                   const unsigned short* __restrict__ kz, const unsigned short* __restrict__ vs,
                                                   const unsigned short* __restrict__ vz, float* __restrict__ part) {
  __shared__ __attribute__((aligned(16))) float lds[L_TOTAL];
  STAMP(64u << 20, 0);
  const int bid = blockIdx.x, pair = bid >> 2, odd = bid & 1;
  const int tid = threadIdx.x, lane = tid & 63, wave = __builtin_amdgcn_readfirstlane(tid >> 6);
  constexpr float C2 = 0.0625f * 1.4426950408889634f;
  const int rbase = bid * REG, r0 = odd ? SKR : 0, r1 = odd ? REG : REG + SKR;
  const int ng = odd ? REG / GRP : NGMAX;
  const int* const kb = qk + (size_t)rbase * DH;
  const int* const vb = qv + (size_t)rbase * DH;
  const __amdgpu_buffer_rsrc_t rk = __builtin_amdgcn_make_buffer_rsrc((void*)kb, 0, r1 * (DH * 4), 0x00020000);
  const __amdgpu_buffer_rsrc_t rv = __builtin_amdgcn_make_buffer_rsrc((void*)vb, 0, r1 * (DH * 4), 0x00020000);
  const int voff = lane * 16 + wave * 1024;

  u32x2 uq[2];
#pragma unroll
  for (int h = 0; h < 2; ++h) uq[h] = __builtin_nontemporal_load(reinterpret_cast<const u32x2*>(q + ((size_t)pair * 2 + h) * DH + 4 * lane));
  constexpr int GPW = (NGMAX + WAVES - 1) / WAVES;
  u32x2 us[GPW], uz[GPW];
#pragma unroll
  for (int i = 0; i < GPW; ++i) {
    const int g = wave + i * WAVES, gc = g < ng ? g : 0;
    const size_t o = ((size_t)(rbase / GRP) + gc) * DH + 4 * lane;
    us[i] = __builtin_nontemporal_load(reinterpret_cast<const u32x2*>(ks + o)); uz[i] = __builtin_nontemporal_load(reinterpret_cast<const u32x2*>(kz + o));
  }
  constexpr int VPT = (TOKMAX / 2 + NTHR - 1) / NTHR;
  unsigned uvs[VPT], uvz[VPT];
#pragma unroll
  for (int i = 0; i < VPT; ++i) {
    const int t2 = tid + i * NTHR, tc = 2 * t2 < r1 ? t2 : 0;
    uvs[i] = __builtin_nontemporal_load(reinterpret_cast<const unsigned*>(vs + rbase + 2 * tc)); uvz[i] = __builtin_nontemporal_load(reinterpret_cast<const unsigned*>(vz + rbase + 2 * tc));
  }

  i32x4 bufA[TT], bufB[TT];
#define LD(dst, rs, row_) do { __builtin_amdgcn_sched_barrier(0); _Pragma("unroll") for (int t_ = 0; t_ < TT; ++t_) \
    dst[t_] = __builtin_bit_cast(i32x4, __builtin_amdgcn_raw_buffer_load_b128(rs, voff, ((row_) + t_ * WAVES) * 1024, 2)); __builtin_amdgcn_sched_barrier(0); } while (0)
  LD(bufA, rk, r0); LD(bufB, rk, r0 + CH);

#pragma unroll
  for (int i = 0; i < GPW; ++i) {
    const int g_ = wave + i * WAVES, g = g_ < NGMAX ? g_ : NGMAX;
    *reinterpret_cast<f32x4*>(lds + L_A + (g * 2) * DH + 4 * lane) = f32x4{bf_lo(uz[i][0]), bf_hi(uz[i][0]), bf_lo(uz[i][1]), bf_hi(uz[i][1])};
    *reinterpret_cast<f32x4*>(lds + L_A + (g * 2 + 1) * DH + 4 * lane) = f32x4{bf_lo(us[i][0]), bf_hi(us[i][0]), bf_lo(us[i][1]), bf_hi(us[i][1])};
  }
#pragma unroll
  for (int i = 0; i < VPT; ++i) {
    const int t2_ = tid + i * NTHR, t2 = t2_ < TOKMAX / 2 ? t2_ : TOKMAX / 2;
    const float s0 = bf_lo(uvs[i]), s1 = bf_hi(uvs[i]);
    *reinterpret_cast<f32x4*>(lds + L_V + 4 * t2) = f32x4{s0, s0 * bf_lo(uvz[i]), s1, s1 * bf_hi(uvz[i])};
  }
  __syncthreads();
  STAMP(64u << 20, 1);

  float* const S = lds + L_S + wave * (JPAD * 2);
  const int swr = ((lane >> TSH) & (TT - 1)) * 2 + (lane >> 5);
  const bool swl = (lane & ((1 << TSH) - 1)) == 0;
  float m = -INFINITY;
#define SCORE(buf, row_, rsn, rown) do { \
    const int g_ = (row_) >> 7; \
    const f32x4 z4 = *reinterpret_cast<const f32x4*>(lds + L_A + (g_ * 2) * DH + 4 * lane); \
    const f32x4 s4 = *reinterpret_cast<const f32x4*>(lds + L_A + (g_ * 2 + 1) * DH + 4 * lane); \
    unsigned d01[TT], d23[TT]; float u[TT]; \
    _Pragma("unroll") for (int t = 0; t < TT; ++t) { \
      d01[t] = pkbf((float)buf[t][0] - z4[0], (float)buf[t][1] - z4[1]); \
      d23[t] = pkbf((float)buf[t][2] - z4[2], (float)buf[t][3] - z4[3]); } \
    if (EARLY) { _Pragma("unroll") for (int t = 0; t < TT; ++t) asm volatile("" : "+v"(d01[t]), "+v"(d23[t])); LD(buf, rsn, rown); } \
    _Pragma("unroll") for (int t = 0; t < TT; ++t) { \
      const unsigned k01 = pkbf(bf_lo(d01[t]) * s4[0], bf_hi(d01[t]) * s4[1]); \
      const unsigned k23 = pkbf(bf_lo(d23[t]) * s4[2], bf_hi(d23[t]) * s4[3]); \
      u[t] = tr32(dot2(k23, uq[0][1], dot2(k01, uq[0][0], 0.f)), dot2(k23, uq[1][1], dot2(k01, uq[1][0], 0.f))); } \
    if (!EARLY) { _Pragma("unroll") for (int t = 0; t < TT; ++t) asm volatile("" : "+v"(u[t])); LD(buf, rsn, rown); } \
    float sc = reduce_scores(u, lane); \
    sc = bf_lo(pkbf(sc, sc)) * C2; \
    m = fmaxf(m, sc); \
    if (swl) S[((row_) - r0) / WAVES * 2 + swr] = sc; } while (0)
#pragma unroll 1
  for (int r = r0; r < r1; r += 2 * CH) {
    const bool lastk = r + 2 * CH >= r1;
    const __amdgpu_buffer_rsrc_t rn = __builtin_amdgcn_make_buffer_rsrc((void*)(lastk ? vb : kb), 0, r1 * (DH * 4), 0x00020000);
    const int rnx = lastk ? r0 : r + 2 * CH;
    SCORE(bufA, r, rn, rnx);
    SCORE(bufB, r + CH, rn, rnx + CH);
  }
  m = half_max(m);
  float l = 0.f, zs = 0.f;
  {
    const int nj = (r1 - r0) / WAVES;
    constexpr int NI = (JMAX + 31) / 32;
    float sc[NI]; f32x2 vsz[NI];
#pragma unroll
    for (int i = 0; i < NI; ++i) {
      const int j = i * 32 + (lane & 31), jc = j < nj ? j : 0;
      sc[i] = S[jc * 2 + (lane >> 5)];
      vsz[i] = *reinterpret_cast<const f32x2*>(lds + L_V + 2 * (r0 + jc * WAVES + wave));
    }
#pragma unroll
    for (int i = 0; i < NI; ++i) {
      const int j = i * 32 + (lane & 31);
      const float pr = j < nj ? __builtin_amdgcn_exp2f(sc[i] - m) : 0.f;
      l += pr; zs = fmaf(pr, vsz[i][1], zs);
      S[j * 2 + (lane >> 5)] = pr * vsz[i][0];
    }
  }
  float acc[2][4] = {{0.f, 0.f, 0.f, 0.f}, {0.f, 0.f, 0.f, 0.f}};
#define ACCUM(buf, row_) do { \
    f32x4 pp[TT / 2]; \
    _Pragma("unroll") for (int t = 0; t < TT / 2; ++t) pp[t] = *reinterpret_cast<const f32x4*>(S + ((row_) - r0) / WAVES * 2 + 4 * t); \
    _Pragma("unroll") for (int t = 0; t < TT; ++t) { \
      const float p0 = pp[t >> 1][(t & 1) * 2], p1 = pp[t >> 1][(t & 1) * 2 + 1]; \
      _Pragma("unroll") for (int e = 0; e < 4; ++e) { const float f = (float)buf[t][e]; acc[0][e] = fmaf(p0, f, acc[0][e]); acc[1][e] = fmaf(p1, f, acc[1][e]); } } \
    asm volatile("" : "+v"(acc[0][0]), "+v"(acc[0][1]), "+v"(acc[0][2]), "+v"(acc[0][3]), "+v"(acc[1][0]), "+v"(acc[1][1]), "+v"(acc[1][2]), "+v"(acc[1][3])); \
    LD(buf, rv, (row_) + 2 * CH); } while (0)
#pragma unroll 1
  for (int r = r0; r < r1; r += 2 * CH) {
    ACCUM(bufA, r);
    ACCUM(bufB, r + CH);
  }
  STAMP(64u << 20, 2);

  {
    const float l_ = half_sum(l), z_ = half_sum(zs);
    const float z0 = rdlane(z_, 0), z1 = rdlane(z_, 32);
    float* rec = lds + L_R + wave * PART;
    *reinterpret_cast<f32x4*>(rec + 4 * lane) = f32x4{acc[0][0] - z0, acc[0][1] - z0, acc[0][2] - z0, acc[0][3] - z0};
    *reinterpret_cast<f32x4*>(rec + DH + 4 * lane) = f32x4{acc[1][0] - z1, acc[1][1] - z1, acc[1][2] - z1, acc[1][3] - z1};
    if ((lane & 31) == 0) { rec[2 * DH + (lane >> 5)] = m; rec[2 * DH + 2 + (lane >> 5)] = l_; }
  }
  __syncthreads();
  float* dst = part + (size_t)bid * PART;
#pragma unroll
  for (int i = tid; i < 2 * DH; i += NTHR) {
    const int h = i >> 8;
    const float* recs = lds + L_R;
    float mm[WAVES], M = -INFINITY;
#pragma unroll
    for (int x = 0; x < WAVES; ++x) { mm[x] = recs[x * PART + 2 * DH + h]; M = fmaxf(M, mm[x]); }
    float o = 0.f, L = 0.f;
#pragma unroll
    for (int x = 0; x < WAVES; ++x) {
      const float sw = __builtin_amdgcn_exp2f(mm[x] - M);
      o = fmaf(sw, recs[x * PART + i], o);
      L = fmaf(sw, recs[x * PART + 2 * DH + 2 + h], L);
    }
    dst[i] = o;
    if ((i & (DH - 1)) < 2) dst[2 * DH + (i & 1) * 2 + h] = (i & 1) ? L : M;
  }
  STAMP(64u << 20, 3);
}

__global__ void __launch_bounds__(256) attn_combine(const float* __restrict__ part, __hip_bfloat16* __restrict__ out) {
  STAMP(65u << 20, 0);
  const int pair = blockIdx.x >> 1, h = blockIdx.x & 1, d = threadIdx.x;
  const float* p = part + (size_t)pair * NSPLIT * PART;
  float mm[NSPLIT], ll[NSPLIT], oo[NSPLIT];
#pragma unroll
  for (int s = 0; s < NSPLIT; ++s) { mm[s] = p[s * PART + 2 * DH + h]; ll[s] = p[s * PART + 2 * DH + 2 + h]; oo[s] = p[s * PART + h * DH + d]; }
  float M = -INFINITY;
#pragma unroll
  for (int s = 0; s < NSPLIT; ++s) M = fmaxf(M, mm[s]);
  float o = 0.f, L = 0.f;
#pragma unroll
  for (int s = 0; s < NSPLIT; ++s) {
    const float sw = __builtin_amdgcn_exp2f(mm[s] - M);
    o = fmaf(sw, oo[s], o);
    L = fmaf(sw, ll[s], L);
  }
  out[(size_t)blockIdx.x * DH + d] = __float2bfloat16(o / L);
  STAMP(65u << 20, 3);
}

}

extern "C" void kernel_launch(void* const* d_in, const int* in_sizes, int n_in, void* d_out, int out_size, void* d_ws,
                              size_t ws_size, hipStream_t stream) {
  const unsigned short* q = (const unsigned short*)d_in[0];
  const int* qk = (const int*)d_in[1];
  const int* qv = (const int*)d_in[2];
  const unsigned short* ks = (const unsigned short*)d_in[3];
  const unsigned short* kz = (const unsigned short*)d_in[4];
  const unsigned short* vs = (const unsigned short*)d_in[5];
  const unsigned short* vz = (const unsigned short*)d_in[6];
  float* part = (float*)d_ws;
  attn_split<<<NWG, NTHR, 0, stream>>>(q, qk, qv, ks, kz, vs, vz, part);
  attn_combine<<<B * HQ, 256, 0, stream>>>(part, (__hip_bfloat16*)d_out);
}
```

```cpp
#include <hip/hip_runtime.h>
#include <hip/hip_bf16.h>
#include <stdint.h>

namespace {

constexpr int B = 8, HQ = 16, HKV = 8, SKV = 4096, DH = 256, GRP = 128;
constexpr int NSPLIT = 4, NWG = B * HKV * NSPLIT, REG = SKV / NSPLIT;
#define WAVES 4
#define TT 4
#define SKR 0
#define PACE 880
#define ANC -1
#define EARLY 0
constexpr int NTHR = WAVES * 64;
constexpr int CH = WAVES * TT;
static_assert(GRP % CH == 0 && SKR % CH == 0 && SKR < GRP && (TT == 2 || TT == 4 || TT == 8), "shape");
constexpr int NGMAX = REG / GRP + (SKR ? 1 : 0);
constexpr int TOKMAX = NGMAX * GRP;
constexpr int JMAX = (REG + SKR) / WAVES;
constexpr int JPAD = (JMAX + TT + 31) / 32 * 32;

typedef int i32x4 __attribute__((ext_vector_type(4)));
typedef float f32x4 __attribute__((ext_vector_type(4)));
typedef float f32x2 __attribute__((ext_vector_type(2)));
typedef unsigned u32x2 __attribute__((ext_vector_type(2)));
typedef __bf16 bf16x2 __attribute__((ext_vector_type(2)));

__device__ __forceinline__ float bf_lo(unsigned u) { return __builtin_bit_cast(float, u << 16); }
__device__ __forceinline__ float bf_hi(unsigned u) { return __builtin_bit_cast(float, u & 0xffff0000u); }
__device__ __forceinline__ float dot2(unsigned a, unsigned b, float c) {
  return __builtin_amdgcn_fdot2_f32_bf16(__builtin_bit_cast(bf16x2, a), __builtin_bit_cast(bf16x2, b), c, false);
}
__device__ __forceinline__ unsigned pkbf(float lo, float hi) {
  bf16x2 r; r[0] = (__bf16)lo; r[1] = (__bf16)hi;
  return __builtin_bit_cast(unsigned, r);
}

template <int CTRL> __device__ __forceinline__ float dpp(float x) {
  return __builtin_bit_cast(float, __builtin_amdgcn_mov_dpp(__builtin_bit_cast(int, x), CTRL, 0xf, 0xf, true));
}
constexpr int XOR1 = 0xB1, XOR2 = 0x4E, XOR7 = 0x141, XOR8 = 0x128;

__device__ __forceinline__ float tr32(float a, float b) {
  auto r = __builtin_amdgcn_permlane32_swap(__float_as_uint(a), __float_as_uint(b), false, false);
  return __uint_as_float(r[0]) + __uint_as_float(r[1]);
}
__device__ __forceinline__ float tr16(float a, float b) {
  auto r = __builtin_amdgcn_permlane16_swap(__float_as_uint(a), __float_as_uint(b), false, false);
  return __uint_as_float(r[0]) + __uint_as_float(r[1]);
}
__device__ __forceinline__ float max16(float x) {
  auto r = __builtin_amdgcn_permlane16_swap(__float_as_uint(x), __float_as_uint(x), false, false);
  return fmaxf(__uint_as_float(r[0]), __uint_as_float(r[1]));
}
template <int CTRL> __device__ __forceinline__ float trrow(float lo, float hi, bool bit) {
  return (bit ? hi : lo) + dpp<CTRL>(bit ? lo : hi);
}
__device__ __forceinline__ float rdlane(float x, int l) {
  return __builtin_bit_cast(float, __builtin_amdgcn_readlane(__builtin_bit_cast(int, x), l));
}
constexpr int TSH = TT == 8 ? 2 : TT == 4 ? 3 : 4;
__device__ __forceinline__ float reduce_scores(float (&u)[TT], int lane) {
#pragma unroll
  for (int t = 0; t < TT / 2; ++t) u[t] = tr16(u[t], u[t + TT / 2]);
  float s;
  if constexpr (TT == 8) {
    const float y0 = trrow<XOR8>(u[0], u[2], (lane & 8) != 0), y1 = trrow<XOR8>(u[1], u[3], (lane & 8) != 0);
    s = trrow<XOR7>(y0, y1, (lane & 4) != 0);
    s += dpp<XOR2>(s); s += dpp<XOR1>(s);
  } else {
    if constexpr (TT == 4) s = trrow<XOR8>(u[0], u[1], (lane & 8) != 0);
    else s = u[0] + dpp<XOR8>(u[0]);
    s += dpp<XOR1>(s); s += dpp<XOR2>(s); s += dpp<XOR7>(s);
  }
  return s;
}
__device__ __forceinline__ float half_max(float x) {
  x = fmaxf(x, dpp<XOR1>(x)); x = fmaxf(x, dpp<XOR2>(x)); x = fmaxf(x, dpp<XOR7>(x)); x = fmaxf(x, dpp<XOR8>(x));
  return max16(x);
}
__device__ __forceinline__ float half_sum(float x) {
  x += dpp<XOR1>(x); x += dpp<XOR2>(x); x += dpp<XOR7>(x); x += dpp<XOR8>(x);
  return tr16(x, x);
}

constexpr int PART = 2 * DH + 4;
constexpr int L_A = 0;
constexpr int L_V = L_A + (NGMAX + 1) * 2 * DH;
constexpr int L_S = L_V + (TOKMAX + 2) * 2;
constexpr int L_R = L_S + WAVES * JPAD * 2;
constexpr int L_TOTAL = L_R + WAVES * PART;

#define TS 0
#define STAMP(base, k) do {} while (0)

__global__ void __launch_bounds__(NTHR) attn_split(const unsigned short* __restrict__ q, const int* __restrict__ qk,
                                                   const int* __restrict__ qv, const unsigned short* __restrict__ ks,
                                                   const unsigned short* __restrict__ kz, const unsigned short* __restrict__ vs,
                                                   const unsigned short* __restrict__ vz, float* __restrict__ part) {
  __shared__ __attribute__((aligned(16))) float lds[L_TOTAL];
  STAMP(64u << 20, 0);
  const unsigned tk0 = (unsigned)__builtin_amdgcn_s_memrealtime();
  const int bid = blockIdx.x, pair = bid >> 2, odd = bid & 1;
  const int tid = threadIdx.x, lane = tid & 63, wave = __builtin_amdgcn_readfirstlane(tid >> 6);
  constexpr float C2 = 0.0625f * 1.4426950408889634f;
  const int rbase = bid * REG, r0 = odd ? SKR : 0, r1 = odd ? REG : REG + SKR;
  const int ng = odd ? REG / GRP : NGMAX;
  const int* const kb = qk + (size_t)rbase * DH;
  const int* const vb = qv + (size_t)rbase * DH;
  const __amdgpu_buffer_rsrc_t rk = __builtin_amdgcn_make_buffer_rsrc((void*)kb, 0, r1 * (DH * 4), 0x00020000);
  const __amdgpu_buffer_rsrc_t rv = __builtin_amdgcn_make_buffer_rsrc((void*)vb, 0, r1 * (DH * 4), 0x00020000);
  const int voff = lane * 16 + wave * 1024;

  u32x2 uq[2];
#pragma unroll
  for (int h = 0; h < 2; ++h) uq[h] = __builtin_nontemporal_load(reinterpret_cast<const u32x2*>(q + ((size_t)pair * 2 + h) * DH + 4 * lane));
  constexpr int GPW = (NGMAX + WAVES - 1) / WAVES;
  u32x2 us[GPW], uz[GPW];
#pragma unroll
  for (int i = 0; i < GPW; ++i) {
    const int g = wave + i * WAVES, gc = g < ng ? g : 0;
    const size_t o = ((size_t)(rbase / GRP) + gc) * DH + 4 * lane;
    us[i] = __builtin_nontemporal_load(reinterpret_cast<const u32x2*>(ks + o)); uz[i] = __builtin_nontemporal_load(reinterpret_cast<const u32x2*>(kz + o));
  }
  constexpr int VPT = (TOKMAX / 2 + NTHR - 1) / NTHR;
  unsigned uvs[VPT], uvz[VPT];
#pragma unroll
  for (int i = 0; i < VPT; ++i) {
    const int t2 = tid + i * NTHR, tc = 2 * t2 < r1 ? t2 : 0;
    uvs[i] = __builtin_nontemporal_load(reinterpret_cast<const unsigned*>(vs + rbase + 2 * tc)); uvz[i] = __builtin_nontemporal_load(reinterpret_cast<const unsigned*>(vz + rbase + 2 * tc));
  }

  i32x4 bufA[TT], bufB[TT];
#define LD(dst, rs, row_) do { __builtin_amdgcn_sched_barrier(0); _Pragma("unroll") for (int t_ = 0; t_ < TT; ++t_) \
    dst[t_] = __builtin_bit_cast(i32x4, __builtin_amdgcn_raw_buffer_load_b128(rs, voff, ((row_) + t_ * WAVES) * 1024, 2)); __builtin_amdgcn_sched_barrier(0); } while (0)
  LD(bufA, rk, r0); LD(bufB, rk, r0 + CH);

#pragma unroll
  for (int i = 0; i < GPW; ++i) {
    const int g_ = wave + i * WAVES, g = g_ < NGMAX ? g_ : NGMAX;
    *reinterpret_cast<f32x4*>(lds + L_A + (g * 2) * DH + 4 * lane) = f32x4{bf_lo(uz[i][0]), bf_hi(uz[i][0]), bf_lo(uz[i][1]), bf_hi(uz[i][1])};
    *reinterpret_cast<f32x4*>(lds + L_A + (g * 2 + 1) * DH + 4 * lane) = f32x4{bf_lo(us[i][0]), bf_hi(us[i][0]), bf_lo(us[i][1]), bf_hi(us[i][1])};
  }
#pragma unroll
  for (int i = 0; i < VPT; ++i) {
    const int t2_ = tid + i * NTHR, t2 = t2_ < TOKMAX / 2 ? t2_ : TOKMAX / 2;
    const float s0 = bf_lo(uvs[i]), s1 = bf_hi(uvs[i]);
    *reinterpret_cast<f32x4*>(lds + L_V + 4 * t2) = f32x4{s0, s0 * bf_lo(uvz[i]), s1, s1 * bf_hi(uvz[i])};
  }
  __syncthreads();
  STAMP(64u << 20, 1);

  float* const S = lds + L_S + wave * (JPAD * 2);
  const int swr = ((lane >> TSH) & (TT - 1)) * 2 + (lane >> 5);
  const bool swl = (lane & ((1 << TSH) - 1)) == 0;
  float m = -INFINITY;
  const unsigned tstart = ANC < 0 ? (unsigned)__builtin_amdgcn_s_memrealtime() : tk0;
  unsigned tgt = ANC < 0 ? 0 : ANC * 16;
#define PACED_LD(buf, rs, row_) do { if (PACE) { while ((((unsigned)__builtin_amdgcn_s_memrealtime() - tstart) << 4) < tgt) __builtin_amdgcn_s_sleep(1); tgt += PACE; } \
    LD(buf, rs, row_); } while (0)
#define SCORE(buf, row_, rsn, rown) do { \
    const bool live_ = SKR % (2 * CH) == 0 || (row_) < r1;        \
    const int g_ = (live_ ? (row_) : r0) >> 7; \
    const f32x4 z4 = *reinterpret_cast<const f32x4*>(lds + L_A + (g_ * 2) * DH + 4 * lane); \
    const f32x4 s4 = *reinterpret_cast<const f32x4*>(lds + L_A + (g_ * 2 + 1) * DH + 4 * lane); \
    unsigned d01[TT], d23[TT]; float u[TT]; \
    _Pragma("unroll") for (int t = 0; t < TT; ++t) { \
      d01[t] = pkbf((float)buf[t][0] - z4[0], (float)buf[t][1] - z4[1]); \
      d23[t] = pkbf((float)buf[t][2] - z4[2], (float)buf[t][3] - z4[3]); } \
    if (EARLY) { _Pragma("unroll") for (int t = 0; t < TT; ++t) asm volatile("" : "+v"(d01[t]), "+v"(d23[t])); PACED_LD(buf, rsn, rown); } \
    _Pragma("unroll") for (int t = 0; t < TT; ++t) { \
      const unsigned k01 = pkbf(bf_lo(d01[t]) * s4[0], bf_hi(d01[t]) * s4[1]); \
      const unsigned k23 = pkbf(bf_lo(d23[t]) * s4[2], bf_hi(d23[t]) * s4[3]); \
      u[t] = tr32(dot2(k23, uq[0][1], dot2(k01, uq[0][0], 0.f)), dot2(k23, uq[1][1], dot2(k01, uq[1][0], 0.f))); } \
    if (!EARLY) { _Pragma("unroll") for (int t = 0; t < TT; ++t) asm volatile("" : "+v"(u[t])); PACED_LD(buf, rsn, rown); } \
    float sc = reduce_scores(u, lane); \
    sc = bf_lo(pkbf(sc, sc)) * C2; \
    if (live_) m = fmaxf(m, sc); \
    if (swl) S[((row_) - r0) / WAVES * 2 + swr] = sc; } while (0)
#pragma unroll 1
  for (int r = r0; r < r1; r += 2 * CH) {
    const bool lastk = r + 2 * CH >= r1;
    const __amdgpu_buffer_rsrc_t rn = __builtin_amdgcn_make_buffer_rsrc((void*)(lastk ? vb : kb), 0, r1 * (DH * 4), 0x00020000);
    const int rnx = lastk ? r0 : r + 2 * CH;
    SCORE(bufA, r, rn, rnx);
    SCORE(bufB, r + CH, rn, rnx + CH);
  }
  m = half_max(m);
  float l = 0.f, zs = 0.f;
  {
    const int nj = (r1 - r0) / WAVES;
    constexpr int NI = SKR % (2 * CH) == 0 ? (JMAX + 31) / 32 : JPAD / 32;
    float sc[NI]; f32x2 vsz[NI];
#pragma unroll
    for (int i = 0; i < NI; ++i) {
      const int j = i * 32 + (lane & 31), jc = j < nj ? j : 0;
      sc[i] = S[jc * 2 + (lane >> 5)];
      vsz[i] = *reinterpret_cast<const f32x2*>(lds + L_V + 2 * (r0 + jc * WAVES + wave));
    }
#pragma unroll
    for (int i = 0; i < NI; ++i) {
      const int j = i * 32 + (lane & 31);
      const float pr = j < nj ? __builtin_amdgcn_exp2f(sc[i] - m) : 0.f;
      l += pr; zs = fmaf(pr, vsz[i][1], zs);
      S[j * 2 + (lane >> 5)] = pr * vsz[i][0];
    }
  }
  float acc[2][4] = {{0.f, 0.f, 0.f, 0.f}, {0.f, 0.f, 0.f, 0.f}};
#define ACCUM(buf, row_) do { \
    f32x4 pp[TT / 2]; \
    _Pragma("unroll") for (int t = 0; t < TT / 2; ++t) pp[t] = *reinterpret_cast<const f32x4*>(S + ((row_) - r0) / WAVES * 2 + 4 * t); \
    _Pragma("unroll") for (int t = 0; t < TT; ++t) { \
      const float p0 = pp[t >> 1][(t & 1) * 2], p1 = pp[t >> 1][(t & 1) * 2 + 1]; \
      _Pragma("unroll") for (int e = 0; e < 4; ++e) { const float f = (float)buf[t][e]; acc[0][e] = fmaf(p0, f, acc[0][e]); acc[1][e] = fmaf(p1, f, acc[1][e]); } } \
    asm volatile("" : "+v"(acc[0][0]), "+v"(acc[0][1]), "+v"(acc[0][2]), "+v"(acc[0][3]), "+v"(acc[1][0]), "+v"(acc[1][1]), "+v"(acc[1][2]), "+v"(acc[1][3])); \
    if ((row_) + 2 * CH >= r1) tgt = 0; \
    PACED_LD(buf, rv, (row_) + 2 * CH); } while (0)
#pragma unroll 1
  for (int r = r0; r < r1; r += 2 * CH) {
    ACCUM(bufA, r);
    ACCUM(bufB, r + CH);
  }
  STAMP(64u << 20, 2);

  {
    const float l_ = half_sum(l), z_ = half_sum(zs);
    const float z0 = rdlane(z_, 0), z1 = rdlane(z_, 32);
    float* rec = lds + L_R + wave * PART;
    *reinterpret_cast<f32x4*>(rec + 4 * lane) = f32x4{acc[0][0] - z0, acc[0][1] - z0, acc[0][2] - z0, acc[0][3] - z0};
    *reinterpret_cast<f32x4*>(rec + DH + 4 * lane) = f32x4{acc[1][0] - z1, acc[1][1] - z1, acc[1][2] - z1, acc[1][3] - z1};
    if ((lane & 31) == 0) { rec[2 * DH + (lane >> 5)] = m; rec[2 * DH + 2 + (lane >> 5)] = l_; }
  }
  __syncthreads();
  float* dst = part + (size_t)bid * PART;
#pragma unroll
  for (int i = tid; i < 2 * DH; i += NTHR) {
    const int h = i >> 8;
    const float* recs = lds + L_R;
    float mm[WAVES], M = -INFINITY;
#pragma unroll
    for (int x = 0; x < WAVES; ++x) { mm[x] = recs[x * PART + 2 * DH + h]; M = fmaxf(M, mm[x]); }
    float o = 0.f, L = 0.f;
#pragma unroll
    for (int x = 0; x < WAVES; ++x) {
      const float sw = __builtin_amdgcn_exp2f(mm[x] - M);
      o = fmaf(sw, recs[x * PART + i], o);
      L = fmaf(sw, recs[x * PART + 2 * DH + 2 + h], L);
    }
    dst[i] = o;
    if ((i & (DH - 1)) < 2) dst[2 * DH + (i & 1) * 2 + h] = (i & 1) ? L : M;
  }
  STAMP(64u << 20, 3);
}

__global__ void __launch_bounds__(256) attn_combine(const float* __restrict__ part, __hip_bfloat16* __restrict__ out) {
  STAMP(65u << 20, 0);
  const int pair = blockIdx.x >> 1, h = blockIdx.x & 1, d = threadIdx.x;
  const float* p = part + (size_t)pair * NSPLIT * PART;
  float mm[NSPLIT], ll[NSPLIT], oo[NSPLIT];
#pragma unroll
  for (int s = 0; s < NSPLIT; ++s) { mm[s] = p[s * PART + 2 * DH + h]; ll[s] = p[s * PART + 2 * DH + 2 + h]; oo[s] = p[s * PART + h * DH + d]; }
  float M = -INFINITY;
#pragma unroll
  for (int s = 0; s < NSPLIT; ++s) M = fmaxf(M, mm[s]);
  float o = 0.f, L = 0.f;
#pragma unroll
  for (int s = 0; s < NSPLIT; ++s) {
    const float sw = __builtin_amdgcn_exp2f(mm[s] - M);
    o = fmaf(sw, oo[s], o);
    L = fmaf(sw, ll[s], L);
  }
  out[(size_t)blockIdx.x * DH + d] = __float2bfloat16(o / L);
  STAMP(65u << 20, 3);
}

}

extern "C" void kernel_launch(void* const* d_in, const int* in_sizes, int n_in, void* d_out, int out_size, void* d_ws,
                              size_t ws_size, hipStream_t stream) {
  const unsigned short* q = (const unsigned short*)d_in[0];
  const int* qk = (const int*)d_in[1];
  const int* qv = (const int*)d_in[2];
  const unsigned short* ks = (const unsigned short*)d_in[3];
  const unsigned short* kz = (const unsigned short*)d_in[4];
  const unsigned short* vs = (const unsigned short*)d_in[5];
  const unsigned short* vz = (const unsigned short*)d_in[6];
  float* part = (float*)d_ws;
  attn_split<<<NWG, NTHR, 0, stream>>>(q, qk, qv, ks, kz, vs, vz, part);
  attn_combine<<<B * HQ, 256, 0, stream>>>(part, (__hip_bfloat16*)d_out);
}
```

```cpp
#include <hip/hip_runtime.h>
#include <hip/hip_bf16.h>
#include <stdint.h>

namespace {

constexpr int B = 8, HQ = 16, HKV = 8, SKV = 4096, DH = 256, GRP = 128;
constexpr int NSPLIT = 4, NWG = B * HKV * NSPLIT, REG = SKV / NSPLIT;
#define WAVES 4
#define TT 4
#define SKR 0
#define PACE 888
#define ANC -1
#define KF 0
#define TAUX 18
#define WEND 0
#define EARLY 0
constexpr int NTHR = WAVES * 64;
constexpr int CH = WAVES * TT;
static_assert(GRP % CH == 0 && SKR % CH == 0 && SKR < GRP && (TT == 2 || TT == 4 || TT == 8), "shape");
constexpr int NGMAX = REG / GRP + (SKR ? 1 : 0);
constexpr int TOKMAX = NGMAX * GRP;
constexpr int JMAX = (REG + SKR) / WAVES;
constexpr int JPAD = (JMAX + TT + 31) / 32 * 32;

typedef int i32x4 __attribute__((ext_vector_type(4)));
typedef float f32x4 __attribute__((ext_vector_type(4)));
typedef float f32x2 __attribute__((ext_vector_type(2)));
typedef unsigned u32x2 __attribute__((ext_vector_type(2)));
typedef unsigned u32x4 __attribute__((ext_vector_type(4)));
typedef unsigned long long u64;
typedef __bf16 bf16x2 __attribute__((ext_vector_type(2)));

__device__ __forceinline__ float bf_lo(unsigned u) { return __builtin_bit_cast(float, u << 16); }
__device__ __forceinline__ float bf_hi(unsigned u) { return __builtin_bit_cast(float, u & 0xffff0000u); }
__device__ __forceinline__ float dot2(unsigned a, unsigned b, float c) {
  return __builtin_amdgcn_fdot2_f32_bf16(__builtin_bit_cast(bf16x2, a), __builtin_bit_cast(bf16x2, b), c, false);
}
__device__ __forceinline__ unsigned pkbf(float lo, float hi) {
  bf16x2 r; r[0] = (__bf16)lo; r[1] = (__bf16)hi;
  return __builtin_bit_cast(unsigned, r);
}

template <int CTRL> __device__ __forceinline__ float dpp(float x) {
  return __builtin_bit_cast(float, __builtin_amdgcn_mov_dpp(__builtin_bit_cast(int, x), CTRL, 0xf, 0xf, true));
}
constexpr int XOR1 = 0xB1, XOR2 = 0x4E, XOR7 = 0x141, XOR8 = 0x128;

__device__ __forceinline__ float tr32(float a, float b) {
  auto r = __builtin_amdgcn_permlane32_swap(__float_as_uint(a), __float_as_uint(b), false, false);
  return __uint_as_float(r[0]) + __uint_as_float(r[1]);
}
__device__ __forceinline__ float tr16(float a, float b) {
  auto r = __builtin_amdgcn_permlane16_swap(__float_as_uint(a), __float_as_uint(b), false, false);
  return __uint_as_float(r[0]) + __uint_as_float(r[1]);
}
__device__ __forceinline__ float max16(float x) {
  auto r = __builtin_amdgcn_permlane16_swap(__float_as_uint(x), __float_as_uint(x), false, false);
  return fmaxf(__uint_as_float(r[0]), __uint_as_float(r[1]));
}
template <int CTRL> __device__ __forceinline__ float trrow(float lo, float hi, bool bit) {
  return (bit ? hi : lo) + dpp<CTRL>(bit ? lo : hi);
}
__device__ __forceinline__ float rdlane(float x, int l) {
  return __builtin_bit_cast(float, __builtin_amdgcn_readlane(__builtin_bit_cast(int, x), l));
}
constexpr int TSH = TT == 8 ? 2 : TT == 4 ? 3 : 4;
__device__ __forceinline__ float reduce_scores(float (&u)[TT], int lane) {
#pragma unroll
  for (int t = 0; t < TT / 2; ++t) u[t] = tr16(u[t], u[t + TT / 2]);
  float s;
  if constexpr (TT == 8) {
    const float y0 = trrow<XOR8>(u[0], u[2], (lane & 8) != 0), y1 = trrow<XOR8>(u[1], u[3], (lane & 8) != 0);
    s = trrow<XOR7>(y0, y1, (lane & 4) != 0);
    s += dpp<XOR2>(s); s += dpp<XOR1>(s);
  } else {
    if constexpr (TT == 4) s = trrow<XOR8>(u[0], u[1], (lane & 8) != 0);
    else s = u[0] + dpp<XOR8>(u[0]);
    s += dpp<XOR1>(s); s += dpp<XOR2>(s); s += dpp<XOR7>(s);
  }
  return s;
}
__device__ __forceinline__ float half_max(float x) {
  x = fmaxf(x, dpp<XOR1>(x)); x = fmaxf(x, dpp<XOR2>(x)); x = fmaxf(x, dpp<XOR7>(x)); x = fmaxf(x, dpp<XOR8>(x));
  return max16(x);
}
__device__ __forceinline__ float half_sum(float x) {
  x += dpp<XOR1>(x); x += dpp<XOR2>(x); x += dpp<XOR7>(x); x += dpp<XOR8>(x);
  return tr16(x, x);
}

constexpr int PART = 2 * DH + 4;
constexpr int L_A = 0;
constexpr int L_V = L_A + (NGMAX + 1) * 2 * DH;
constexpr int L_S = L_V + (TOKMAX + 2) * 2;
constexpr int L_R = L_S + WAVES * JPAD * 2;
constexpr int L_T = L_R + WAVES * PART;
constexpr int L_TOTAL = L_T + 8 * 65 * 2;

static_assert(WAVES == NSPLIT && NTHR == 256, "wave w of every split owns output slice w");
constexpr int MBOX = 1152;
constexpr int MPAIR = NSPLIT * NSPLIT * MBOX;
constexpr u64 TAG = 0x5A3C96E17Bull;
#define RLX __ATOMIC_RELAXED, __HIP_MEMORY_SCOPE_AGENT
__device__ __forceinline__ void init_word(u64* w) {
  u64 v = __hip_atomic_load(w, RLX);
  if ((v >> 24) != TAG) __hip_atomic_compare_exchange_strong(w, &v, TAG << 24, __ATOMIC_RELAXED, RLX);
}
__device__ __forceinline__ u64 add_word(u64* w, u64 inc) {
  for (;;) {
    const u64 old = __hip_atomic_fetch_add(w, inc, RLX);
    if ((old >> 24) == TAG) return old;
    init_word(w);
  }
}

#define TS 0
#define STAMP(base, k) do {} while (0)

__global__ void __launch_bounds__(NTHR + 64) attn_kernel(const unsigned short* __restrict__ q, const int* __restrict__ qk,
                                                   const int* __restrict__ qv, const unsigned short* __restrict__ ks,
                                                   const unsigned short* __restrict__ kz, const unsigned short* __restrict__ vs,
                                                   const unsigned short* __restrict__ vz, char* part, u64* ctr,
                                                   __hip_bfloat16* __restrict__ out) {
  __shared__ __attribute__((aligned(16))) float lds[L_TOTAL];
  STAMP(64u << 20, 0);
  const unsigned tk0 = (unsigned)__builtin_amdgcn_s_memrealtime();
  const int bid = blockIdx.x, pair = bid >> 2, odd = bid & 1;
  const int tid = threadIdx.x, lane = tid & 63, wave = __builtin_amdgcn_readfirstlane(tid >> 6);
  constexpr float C2 = 0.0625f * 1.4426950408889634f;
  const int slot = bid & 3;
  if (wave == WAVES) {
    __syncthreads();
    u64* W = ctr + pair * 32;
    const __amdgpu_buffer_rsrc_t rp = __builtin_amdgcn_make_buffer_rsrc((void*)(part + (size_t)pair * MPAIR), 0, MPAIR, 0x00020000);
    u32x4 ta[8], tb[8];
#pragma unroll
    for (int e = 0; e < 8; ++e) {
      const int x = e & 3, so = x == slot ? MPAIR : (e < 4 ? (x * NSPLIT + slot) : (slot * NSPLIT + x)) * MBOX;
      ta[e] = __builtin_bit_cast(u32x4, __builtin_amdgcn_raw_buffer_load_b128(rp, lane * 16, so, TAUX));
      tb[e] = __builtin_bit_cast(u32x4, __builtin_amdgcn_raw_buffer_load_b128(rp, 1024, so, TAUX));
    }
    unsigned* T = reinterpret_cast<unsigned*>(lds + L_T);
#pragma unroll
    for (int e = 0; e < 8; ++e) {
      *reinterpret_cast<u32x2*>(T + (e * 65 + lane) * 2) = u32x2{ta[e][1] + 1, ta[e][3] + 1};
      if (lane == 0) *reinterpret_cast<u32x2*>(T + (e * 65 + 64) * 2) = u32x2{tb[e][1] + 1, tb[e][3] + 1};
    }
    asm volatile("s_waitcnt vmcnt(0)" ::: "memory");
    if (lane == 0) {
      init_word(W);
      u64 v = add_word(W, 1ull) + 1;
      const u64 t0 = __builtin_amdgcn_s_memrealtime();
      while ((v & 0xFFFF) != NSPLIT && __builtin_amdgcn_s_memrealtime() - t0 < 20000000ull) {
        __builtin_amdgcn_s_sleep(32);
        v = __hip_atomic_load(W, RLX);
      }
      const u64 o = __hip_atomic_fetch_add(W, 1ull << 16, RLX);
      if (((o >> 16) & 0xFF) == NSPLIT - 1) __hip_atomic_store(W, 0ull, RLX);
    }
    __syncthreads();
    return;
  }
  const __amdgpu_buffer_rsrc_t rm = __builtin_amdgcn_make_buffer_rsrc((void*)(part + (size_t)pair * MPAIR + wave * (NSPLIT * MBOX)), 0, NSPLIT * MBOX, 0x00020000);
  const int rbase = bid * REG, r0 = odd ? SKR : 0, r1 = odd ? REG : REG + SKR;
  const int ng = odd ? REG / GRP : NGMAX;
  const int* const kb = qk + (size_t)rbase * DH;
  const int* const vb = qv + (size_t)rbase * DH;
  const __amdgpu_buffer_rsrc_t rk = __builtin_amdgcn_make_buffer_rsrc((void*)kb, 0, r1 * (DH * 4), 0x00020000);
  const __amdgpu_buffer_rsrc_t rv = __builtin_amdgcn_make_buffer_rsrc((void*)vb, 0, r1 * (DH * 4), 0x00020000);
  const int voff = lane * 16 + wave * 1024;

  i32x4 bufA[TT], bufB[TT];
#define LD(dst, rs, row_) do { __builtin_amdgcn_sched_barrier(0); _Pragma("unroll") for (int t_ = 0; t_ < TT; ++t_) \
    dst[t_] = __builtin_bit_cast(i32x4, __builtin_amdgcn_raw_buffer_load_b128(rs, voff, ((row_) + t_ * WAVES) * 1024, 2)); __builtin_amdgcn_sched_barrier(0); } while (0)
  u32x2 uq[2];
#pragma unroll
  for (int h = 0; h < 2; ++h) uq[h] = __builtin_nontemporal_load(reinterpret_cast<const u32x2*>(q + ((size_t)pair * 2 + h) * DH + 4 * lane));
  constexpr int GPW = (NGMAX + WAVES - 1) / WAVES;
  u32x2 us[GPW], uz[GPW];
#pragma unroll
  for (int i = 0; i < GPW; ++i) {
    const int g = wave + i * WAVES, gc = g < ng ? g : 0;
    const size_t o = ((size_t)(rbase / GRP) + gc) * DH + 4 * lane;
    us[i] = __builtin_nontemporal_load(reinterpret_cast<const u32x2*>(ks + o)); uz[i] = __builtin_nontemporal_load(reinterpret_cast<const u32x2*>(kz + o));
  }
  constexpr int VPT = (TOKMAX / 2 + NTHR - 1) / NTHR;
  unsigned uvs[VPT], uvz[VPT];
#pragma unroll
  for (int i = 0; i < VPT; ++i) {
    const int t2 = tid + i * NTHR, tc = 2 * t2 < r1 ? t2 : 0;
    uvs[i] = __builtin_nontemporal_load(reinterpret_cast<const unsigned*>(vs + rbase + 2 * tc)); uvz[i] = __builtin_nontemporal_load(reinterpret_cast<const unsigned*>(vz + rbase + 2 * tc));
  }

  LD(bufA, rk, r0); LD(bufB, rk, r0 + CH);

#pragma unroll
  for (int i = 0; i < GPW; ++i) {
    const int g_ = wave + i * WAVES, g = g_ < NGMAX ? g_ : NGMAX;
    *reinterpret_cast<f32x4*>(lds + L_A + (g * 2) * DH + 4 * lane) = f32x4{bf_lo(uz[i][0]), bf_hi(uz[i][0]), bf_lo(uz[i][1]), bf_hi(uz[i][1])};
    *reinterpret_cast<f32x4*>(lds + L_A + (g * 2 + 1) * DH + 4 * lane) = f32x4{bf_lo(us[i][0]), bf_hi(us[i][0]), bf_lo(us[i][1]), bf_hi(us[i][1])};
  }
#pragma unroll
  for (int i = 0; i < VPT; ++i) {
    const int t2_ = tid + i * NTHR, t2 = t2_ < TOKMAX / 2 ? t2_ : TOKMAX / 2;
    const float s0 = bf_lo(uvs[i]), s1 = bf_hi(uvs[i]);
    *reinterpret_cast<f32x4*>(lds + L_V + 4 * t2) = f32x4{s0, s0 * bf_lo(uvz[i]), s1, s1 * bf_hi(uvz[i])};
  }
  __syncthreads();
  STAMP(64u << 20, 1);

  float* const S = lds + L_S + wave * (JPAD * 2);
  const int swr = ((lane >> TSH) & (TT - 1)) * 2 + (lane >> 5);
  const bool swl = (lane & ((1 << TSH) - 1)) == 0;
  float m = -INFINITY;
  const unsigned tstart = ANC < 0 ? (unsigned)__builtin_amdgcn_s_memrealtime() : tk0;
  unsigned tgt = ANC < 0 ? 0 : ANC * 16;
#define DCAP 0
#define PACED_LD(buf, rs, row_) do { if (PACE) { unsigned now_; \
      while ((now_ = ((unsigned)__builtin_amdgcn_s_memrealtime() - tstart) << 4) < tgt) __builtin_amdgcn_s_sleep(1); \
      if (DCAP && now_ > tgt + DCAP * 16) tgt = now_ - DCAP * 16; \
      tgt += PACE; } \
    LD(buf, rs, row_); } while (0)
#define SCORE(buf, row_, rsn, rown) do { \
    const bool live_ = SKR % (2 * CH) == 0 || (row_) < r1;        \
    const int g_ = (live_ ? (row_) : r0) >> 7; \
    const f32x4 z4 = *reinterpret_cast<const f32x4*>(lds + L_A + (g_ * 2) * DH + 4 * lane); \
    const f32x4 s4 = *reinterpret_cast<const f32x4*>(lds + L_A + (g_ * 2 + 1) * DH + 4 * lane); \
    unsigned d01[TT], d23[TT]; float u[TT]; \
    _Pragma("unroll") for (int t = 0; t < TT; ++t) { \
      d01[t] = pkbf((float)buf[t][0] - z4[0], (float)buf[t][1] - z4[1]); \
      d23[t] = pkbf((float)buf[t][2] - z4[2], (float)buf[t][3] - z4[3]); } \
    if (EARLY) { _Pragma("unroll") for (int t = 0; t < TT; ++t) asm volatile("" : "+v"(d01[t]), "+v"(d23[t])); PACED_LD(buf, rsn, rown); } \
    _Pragma("unroll") for (int t = 0; t < TT; ++t) { \
      const unsigned k01 = pkbf(bf_lo(d01[t]) * s4[0], bf_hi(d01[t]) * s4[1]); \
      const unsigned k23 = pkbf(bf_lo(d23[t]) * s4[2], bf_hi(d23[t]) * s4[3]); \
      u[t] = tr32(dot2(k23, uq[0][1], dot2(k01, uq[0][0], 0.f)), dot2(k23, uq[1][1], dot2(k01, uq[1][0], 0.f))); } \
    if (!EARLY) { _Pragma("unroll") for (int t = 0; t < TT; ++t) asm volatile("" : "+v"(u[t])); PACED_LD(buf, rsn, rown); } \
    float sc = reduce_scores(u, lane); \
    sc = bf_lo(pkbf(sc, sc)) * C2; \
    if (live_) m = fmaxf(m, sc); \
    if (swl) S[((row_) - r0) / WAVES * 2 + swr] = sc; } while (0)
#pragma unroll 1
  for (int r = r0; r < r1; r += 2 * CH) {
    const bool lastk = r + 2 * CH >= r1;
    const __amdgpu_buffer_rsrc_t rn = __builtin_amdgcn_make_buffer_rsrc((void*)(lastk ? vb : kb), 0, r1 * (DH * 4), 0x00020000);
    const int rnx = lastk ? r0 : r + 2 * CH;
    SCORE(bufA, r, rn, rnx);
    SCORE(bufB, r + CH, rn, rnx + CH);
  }
  m = half_max(m);
  float l = 0.f, zs = 0.f;
  {
    const int nj = (r1 - r0) / WAVES;
    constexpr int NI = SKR % (2 * CH) == 0 ? (JMAX + 31) / 32 : JPAD / 32;
    float sc[NI]; f32x2 vsz[NI];
#pragma unroll
    for (int i = 0; i < NI; ++i) {
      const int j = i * 32 + (lane & 31), jc = j < nj ? j : 0;
      sc[i] = S[jc * 2 + (lane >> 5)];
      vsz[i] = *reinterpret_cast<const f32x2*>(lds + L_V + 2 * (r0 + jc * WAVES + wave));
    }
#pragma unroll
    for (int i = 0; i < NI; ++i) {
      const int j = i * 32 + (lane & 31);
      const float pr = j < nj ? __builtin_amdgcn_exp2f(sc[i] - m) : 0.f;
      l += pr; zs = fmaf(pr, vsz[i][1], zs);
      S[j * 2 + (lane >> 5)] = pr * vsz[i][0];
    }
  }
  float acc[2][4] = {{0.f, 0.f, 0.f, 0.f}, {0.f, 0.f, 0.f, 0.f}};
#define ACCUM(buf, row_) do { \
    f32x4 pp[TT / 2]; \
    _Pragma("unroll") for (int t = 0; t < TT / 2; ++t) pp[t] = *reinterpret_cast<const f32x4*>(S + ((row_) - r0) / WAVES * 2 + 4 * t); \
    _Pragma("unroll") for (int t = 0; t < TT; ++t) { \
      const float p0 = pp[t >> 1][(t & 1) * 2], p1 = pp[t >> 1][(t & 1) * 2 + 1]; \
      _Pragma("unroll") for (int e = 0; e < 4; ++e) { const float f = (float)buf[t][e]; acc[0][e] = fmaf(p0, f, acc[0][e]); acc[1][e] = fmaf(p1, f, acc[1][e]); } } \
    asm volatile("" : "+v"(acc[0][0]), "+v"(acc[0][1]), "+v"(acc[0][2]), "+v"(acc[0][3]), "+v"(acc[1][0]), "+v"(acc[1][1]), "+v"(acc[1][2]), "+v"(acc[1][3])); \
    if ((row_) + 2 * CH >= r1) tgt = 0; \
    PACED_LD(buf, rv, (row_) + 2 * CH); } while (0)
#pragma unroll 1
  for (int r = r0; r < r1; r += 2 * CH) {
    ACCUM(bufA, r);
    ACCUM(bufB, r + CH);
  }
  STAMP(64u << 20, 2);

  {
    const float l_ = half_sum(l), z_ = half_sum(zs);
    const float z0 = rdlane(z_, 0), z1 = rdlane(z_, 32);
    float* rec = lds + L_R + wave * PART;
    *reinterpret_cast<f32x4*>(rec + 4 * lane) = f32x4{acc[0][0] - z0, acc[0][1] - z0, acc[0][2] - z0, acc[0][3] - z0};
    *reinterpret_cast<f32x4*>(rec + DH + 4 * lane) = f32x4{acc[1][0] - z1, acc[1][1] - z1, acc[1][2] - z1, acc[1][3] - z1};
    if ((lane & 31) == 0) { rec[2 * DH + (lane >> 5)] = m; rec[2 * DH + 2 + (lane >> 5)] = l_; }
  }
  __syncthreads();
  const int e0 = tid * 2, h = wave >> 1;
  float M = -INFINITY, L = 0.f, o0 = 0.f, o1 = 0.f;
  {
    const float* recs = lds + L_R;
    float mm[WAVES];
#pragma unroll
    for (int x = 0; x < WAVES; ++x) { mm[x] = recs[x * PART + 2 * DH + h]; M = fmaxf(M, mm[x]); }
#pragma unroll
    for (int x = 0; x < WAVES; ++x) {
      const float sw = __builtin_amdgcn_exp2f(mm[x] - M);
      const f32x2 v = *reinterpret_cast<const f32x2*>(recs + x * PART + e0);
      o0 = fmaf(sw, v[0], o0); o1 = fmaf(sw, v[1], o1);
      L = fmaf(sw, recs[x * PART + 2 * DH + 2 + h], L);
    }
  }
  if (wave != slot) {
    const unsigned* T = reinterpret_cast<const unsigned*>(lds + L_T);
    const u32x2 t01 = *reinterpret_cast<const u32x2*>(T + (wave * 65 + lane) * 2), t23 = *reinterpret_cast<const u32x2*>(T + (wave * 65 + 64) * 2);
    const unsigned t4[4] = {t01[0], t01[1], t23[0], t23[1]};
    __builtin_amdgcn_raw_buffer_store_b128(u32x4{__float_as_uint(o0), t4[0], __float_as_uint(o1), t4[1]}, rm, lane * 16, slot * MBOX, 16);
    if (lane == 0) __builtin_amdgcn_raw_buffer_store_b128(u32x4{__float_as_uint(M), t4[2], __float_as_uint(L), t4[3]}, rm, 1024, slot * MBOX, 16);
  } else {
    u32x4 ga[NSPLIT], gb[NSPLIT];
    unsigned tg[NSPLIT][4];
    const unsigned* T = reinterpret_cast<const unsigned*>(lds + L_T);
#pragma unroll
    for (int j = 0; j < NSPLIT; ++j) {
      const u32x2 t01 = *reinterpret_cast<const u32x2*>(T + ((4 + j) * 65 + lane) * 2), t23 = *reinterpret_cast<const u32x2*>(T + ((4 + j) * 65 + 64) * 2);
      tg[j][0] = t01[0]; tg[j][1] = t01[1]; tg[j][2] = t23[0]; tg[j][3] = t23[1];
    }
    const u64 t0 = __builtin_amdgcn_s_memrealtime();
    for (;;) {
      bool ok = true;
#pragma unroll
      for (int j = 0; j < NSPLIT; ++j) {
        const int so = j != slot ? j * MBOX : NSPLIT * MBOX;
        ga[j] = __builtin_bit_cast(u32x4, __builtin_amdgcn_raw_buffer_load_b128(rm, lane * 16, so, 16));
        gb[j] = __builtin_bit_cast(u32x4, __builtin_amdgcn_raw_buffer_load_b128(rm, 1024, so, 16));
      }
#pragma unroll
      for (int j = 0; j < NSPLIT; ++j)
        ok = ok && (j == slot || (ga[j][1] == tg[j][0] && ga[j][3] == tg[j][1] && gb[j][1] == tg[j][2] && gb[j][3] == tg[j][3]));
      if (__all(ok) || __builtin_amdgcn_s_memrealtime() - t0 > 20000000ull) break;
    }
    float Ms[NSPLIT], Mx = -INFINITY;
#pragma unroll
    for (int j = 0; j < NSPLIT; ++j) { Ms[j] = j == slot ? M : __uint_as_float(gb[j][0]); Mx = fmaxf(Mx, Ms[j]); }
    float a0 = 0.f, a1 = 0.f, LL = 0.f;
#pragma unroll
    for (int j = 0; j < NSPLIT; ++j) {
      const float sw = __builtin_amdgcn_exp2f(Ms[j] - Mx);
      a0 = fmaf(sw, j == slot ? o0 : __uint_as_float(ga[j][0]), a0);
      a1 = fmaf(sw, j == slot ? o1 : __uint_as_float(ga[j][2]), a1);
      LL = fmaf(sw, j == slot ? L : __uint_as_float(gb[j][2]), LL);
    }
    *reinterpret_cast<unsigned*>(out + (size_t)pair * 2 * DH + e0) = pkbf(a0 / LL, a1 / LL);
  }
}

}

extern "C" void kernel_launch(void* const* d_in, const int* in_sizes, int n_in, void* d_out, int out_size, void* d_ws,
                              size_t ws_size, hipStream_t stream) {
  const unsigned short* q = (const unsigned short*)d_in[0];
  const int* qk = (const int*)d_in[1];
  const int* qv = (const int*)d_in[2];
  const unsigned short* ks = (const unsigned short*)d_in[3];
  const unsigned short* kz = (const unsigned short*)d_in[4];
  const unsigned short* vs = (const unsigned short*)d_in[5];
  const unsigned short* vz = (const unsigned short*)d_in[6];
  if (WEND) d_ws = (char*)d_ws + ((ws_size - ((size_t)WEND << 20)) & ~(size_t)4095);
  char* part = (char*)d_ws;
  u64* ctr = (u64*)((char*)d_ws + (2u << 20));
  attn_kernel<<<NWG, NTHR + 64, 0, stream>>>(q, qk, qv, ks, kz, vs, vz, part, ctr, (__hip_bfloat16*)d_out);
}
```

```cpp
#include <hip/hip_runtime.h>
#include <hip/hip_bf16.h>
#include <stdint.h>

namespace {

constexpr int B = 8, HQ = 16, HKV = 8, SKV = 4096, DH = 256, GRP = 128;
constexpr int NSPLIT = 4, NWG = B * HKV * NSPLIT, REG = SKV / NSPLIT;
constexpr int WAVES = 4;
constexpr int TT = 4;
constexpr int NTHR = WAVES * 64;
constexpr int CH = WAVES * TT;
constexpr int NG = REG / GRP;
constexpr int JN = REG / WAVES;
static_assert(HQ == 2 * HKV && DH == 256 && WAVES == NSPLIT && GRP % (2 * CH) == 0 && JN % 32 == 0, "shape");
#define PACE 896
#define PACEV 880
#define JUMP 200

typedef int i32x4 __attribute__((ext_vector_type(4)));
typedef float f32x4 __attribute__((ext_vector_type(4)));
typedef float f32x2 __attribute__((ext_vector_type(2)));
typedef unsigned u32x2 __attribute__((ext_vector_type(2)));
typedef unsigned u32x4 __attribute__((ext_vector_type(4)));
typedef unsigned long long u64;
typedef __bf16 bf16x2 __attribute__((ext_vector_type(2)));

__device__ __forceinline__ float bf_lo(unsigned u) { return __builtin_bit_cast(float, u << 16); }
__device__ __forceinline__ float bf_hi(unsigned u) { return __builtin_bit_cast(float, u & 0xffff0000u); }
__device__ __forceinline__ f32x4 bf4(u32x2 u) { return f32x4{bf_lo(u[0]), bf_hi(u[0]), bf_lo(u[1]), bf_hi(u[1])}; }
__device__ __forceinline__ float dot2(unsigned a, unsigned b, float c) {
  return __builtin_amdgcn_fdot2_f32_bf16(__builtin_bit_cast(bf16x2, a), __builtin_bit_cast(bf16x2, b), c, false);
}
__device__ __forceinline__ unsigned pkbf(float lo, float hi) {
  bf16x2 r; r[0] = (__bf16)lo; r[1] = (__bf16)hi;
  return __builtin_bit_cast(unsigned, r);
}

template <int CTRL> __device__ __forceinline__ float dpp(float x) {
  return __builtin_bit_cast(float, __builtin_amdgcn_mov_dpp(__builtin_bit_cast(int, x), CTRL, 0xf, 0xf, true));
}
constexpr int XOR1 = 0xB1, XOR2 = 0x4E, XOR7 = 0x141, XOR8 = 0x128;

__device__ __forceinline__ float tr32(float a, float b) {
  auto r = __builtin_amdgcn_permlane32_swap(__float_as_uint(a), __float_as_uint(b), false, false);
  return __uint_as_float(r[0]) + __uint_as_float(r[1]);
}
__device__ __forceinline__ float tr16(float a, float b) {
  auto r = __builtin_amdgcn_permlane16_swap(__float_as_uint(a), __float_as_uint(b), false, false);
  return __uint_as_float(r[0]) + __uint_as_float(r[1]);
}
__device__ __forceinline__ float rdlane(float x, int l) {
  return __builtin_bit_cast(float, __builtin_amdgcn_readlane(__builtin_bit_cast(int, x), l));
}
__device__ __forceinline__ float half_max(float x) {
  x = fmaxf(x, dpp<XOR1>(x)); x = fmaxf(x, dpp<XOR2>(x)); x = fmaxf(x, dpp<XOR7>(x)); x = fmaxf(x, dpp<XOR8>(x));
  auto r = __builtin_amdgcn_permlane16_swap(__float_as_uint(x), __float_as_uint(x), false, false);
  return fmaxf(__uint_as_float(r[0]), __uint_as_float(r[1]));
}
__device__ __forceinline__ float half_sum(float x) {
  x += dpp<XOR1>(x); x += dpp<XOR2>(x); x += dpp<XOR7>(x); x += dpp<XOR8>(x);
  return tr16(x, x);
}

constexpr int PART = 2 * DH + 4;
constexpr int L_A = 0;
constexpr int L_V = L_A + NG * DH;
constexpr int L_S = L_V + REG * 2;
constexpr int L_R = L_S + WAVES * JN * 2;
constexpr int L_T = L_R + WAVES * PART;
constexpr int L_TOTAL = L_T + 8 * 65 * 2;

constexpr int MBOX = 1152;
constexpr int MPAIR = NSPLIT * NSPLIT * MBOX;
constexpr u64 TAG = 0x5A3C96E17Bull;
#define RLX __ATOMIC_RELAXED, __HIP_MEMORY_SCOPE_AGENT
__device__ __forceinline__ void init_word(u64* w) {
  u64 v = __hip_atomic_load(w, RLX);
  if ((v >> 24) != TAG) __hip_atomic_compare_exchange_strong(w, &v, TAG << 24, __ATOMIC_RELAXED, RLX);
}
__device__ __forceinline__ u64 add_word(u64* w, u64 inc) {
  for (;;) {
    const u64 old = __hip_atomic_fetch_add(w, inc, RLX);
    if ((old >> 24) == TAG) return old;
    init_word(w);
  }
}
#define WAVE_SYNC() do { __builtin_amdgcn_fence(__ATOMIC_RELEASE, "wavefront"); __builtin_amdgcn_wave_barrier(); __builtin_amdgcn_fence(__ATOMIC_ACQUIRE, "wavefront"); } while (0)
constexpr u64 SPIN_LIMIT = 20000000ull;

__global__ void __launch_bounds__(NTHR + 64, 2) attn_kernel(const unsigned short* __restrict__ q, const int* __restrict__ qk,
                                                         const int* __restrict__ qv, const unsigned short* __restrict__ ks,
                                                         const unsigned short* __restrict__ kz, const unsigned short* __restrict__ vs,
                                                         const unsigned short* __restrict__ vz, char* mbox, u64* ctr,
                                                         __hip_bfloat16* __restrict__ out) {
  __shared__ __attribute__((aligned(16))) float lds[L_TOTAL];
  const int bid = blockIdx.x, pair = bid >> 2, slot = bid & 3;
  const int tid = threadIdx.x, lane = tid & 63, wave = __builtin_amdgcn_readfirstlane(tid >> 6);
  const unsigned short* const ksb = ks + (size_t)bid * (NG * DH);
  const unsigned short* const kzb = kz + (size_t)bid * (NG * DH);

  if (wave == WAVES) {
    {
      u32x4 a[NG / 2], z[NG / 2], s8[2], z8[2];
#pragma unroll
      for (int i = 0; i < NG / 2; ++i) {
        z[i] = __builtin_nontemporal_load(reinterpret_cast<const u32x4*>(kzb + i * 512 + lane * 8));
        a[i] = __builtin_nontemporal_load(reinterpret_cast<const u32x4*>(ksb + i * 512 + lane * 8));
      }
#pragma unroll
      for (int i = 0; i < 2; ++i) {
        s8[i] = __builtin_nontemporal_load(reinterpret_cast<const u32x4*>(vs + (size_t)bid * REG + i * 512 + lane * 8));
        z8[i] = __builtin_nontemporal_load(reinterpret_cast<const u32x4*>(vz + (size_t)bid * REG + i * 512 + lane * 8));
      }
#pragma unroll
      for (int i = 0; i < NG / 2; ++i) {
        float* t = lds + L_A + (2 * i + (lane >> 5)) * DH + (lane & 31) * 4;
        *reinterpret_cast<u32x4*>(t) = z[i]; *reinterpret_cast<u32x4*>(t + DH / 2) = a[i];
      }
#pragma unroll
      for (int i = 0; i < 2; ++i)
#pragma unroll
        for (int e = 0; e < 4; ++e) {
          const float s0 = bf_lo(s8[i][e]), s1 = bf_hi(s8[i][e]);
          *reinterpret_cast<f32x4*>(lds + L_V + (i * 512 + lane * 8 + e * 2) * 2) = f32x4{s0, s0 * bf_lo(z8[i][e]), s1, s1 * bf_hi(z8[i][e])};
        }
    }
    __syncthreads();
    u64* W = ctr + pair * 32;
    const __amdgpu_buffer_rsrc_t rp = __builtin_amdgcn_make_buffer_rsrc((void*)(mbox + (size_t)pair * MPAIR), 0, MPAIR, 0x00020000);
    unsigned ta[8], tb[8], tc[8];
#pragma unroll
    for (int e = 0; e < 8; ++e) {
      const int x = e & 3, so = x == slot ? MPAIR : (e < 4 ? (x * NSPLIT + slot) : (slot * NSPLIT + x)) * MBOX;
      ta[e] = __builtin_amdgcn_raw_buffer_load_b32(rp, lane * 8 + 4, so, 18);
      tb[e] = __builtin_amdgcn_raw_buffer_load_b32(rp, lane * 8 + 516, so, 18);
      tc[e] = __builtin_amdgcn_raw_buffer_load_b32(rp, (lane & 1) * 8 + 1028, so, 18);
    }
    unsigned* T = reinterpret_cast<unsigned*>(lds + L_T);
#pragma unroll
    for (int e = 0; e < 8; ++e) {
      *reinterpret_cast<u32x2*>(T + (e * 65 + lane) * 2) = u32x2{ta[e] + 1, tb[e] + 1};
      if (lane < 2) T[(e * 65 + 64) * 2 + lane] = tc[e] + 1;
    }
    asm volatile("s_waitcnt vmcnt(0)" ::: "memory");
    if (lane == 0) {
      init_word(W);
      u64 v = add_word(W, 1ull) + 1;
      const u64 t0 = __builtin_amdgcn_s_memrealtime();
      while ((v & 0xFFFF) != NSPLIT && __builtin_amdgcn_s_memrealtime() - t0 < SPIN_LIMIT) {
        __builtin_amdgcn_s_sleep(32);
        v = __hip_atomic_load(W, RLX);
      }
      const u64 o = __hip_atomic_fetch_add(W, 1ull << 16, RLX);
      if (((o >> 16) & 0xFF) == NSPLIT - 1) __hip_atomic_store(W, 0ull, RLX);
    }
    __syncthreads();
    return;
  }

  const int* const kb = qk + (size_t)bid * (REG * DH);
  const int* const vb = qv + (size_t)bid * (REG * DH);
  const __amdgpu_buffer_rsrc_t rk = __builtin_amdgcn_make_buffer_rsrc((void*)kb, 0, REG * DH * 4, 0x00020000);
  const __amdgpu_buffer_rsrc_t rv = __builtin_amdgcn_make_buffer_rsrc((void*)vb, 0, REG * DH * 4, 0x00020000);
  const int voff = lane * 16 + wave * 1024;
  u32x2 uq[2];
#pragma unroll
  for (int h = 0; h < 2; ++h) uq[h] = __builtin_nontemporal_load(reinterpret_cast<const u32x2*>(q + ((size_t)pair * 2 + h) * DH + 4 * lane));
  u32x2 uz = __builtin_nontemporal_load(reinterpret_cast<const u32x2*>(kzb + 4 * lane));
  u32x2 us = __builtin_nontemporal_load(reinterpret_cast<const u32x2*>(ksb + 4 * lane));
  i32x4 bufA[TT], bufB[TT];
#define LD(dst, rs, row_) do { __builtin_amdgcn_sched_barrier(0); _Pragma("unroll") for (int t_ = 0; t_ < TT; ++t_) \
    dst[t_] = __builtin_bit_cast(i32x4, __builtin_amdgcn_raw_buffer_load_b128(rs, voff, ((row_) + t_ * WAVES) * 1024, 2)); __builtin_amdgcn_sched_barrier(0); } while (0)
  LD(bufA, rk, 0); LD(bufB, rk, CH);
  asm volatile("" : "+v"(uq[0]), "+v"(uq[1]), "+v"(uz), "+v"(us), "+v"(bufB[TT - 1]));

  const unsigned tstart = (unsigned)__builtin_amdgcn_s_memrealtime();
  unsigned tgt = 0, tprev = 0;
#define PACED_LD(buf, rs, row_, pace_) do { unsigned now_; \
    while ((now_ = ((unsigned)__builtin_amdgcn_s_memrealtime() - tstart) << 4) < tgt) __builtin_amdgcn_s_sleep(1); \
    if (now_ - tprev > JUMP * 16 && now_ > 1000 * 16) tgt += now_ - tprev - (pace_); \
    tprev = now_; tgt += (pace_); \
    LD(buf, rs, row_); } while (0)

  constexpr float C2 = 0.0625f * 1.4426950408889634f;
  float* const S = lds + L_S + wave * (JN * 2);
  const int swr = ((lane >> 3) & 3) * 2 + (lane >> 5);
  const bool b3 = (lane & 8) != 0;
  float m = -INFINITY;
#define SCORE(buf, row_, rsn, rown) do { \
    float u[TT]; \
    _Pragma("unroll") for (int t = 0; t < TT; ++t) {       \
      const unsigned d01 = pkbf((float)buf[t][0] - z4[0], (float)buf[t][1] - z4[1]); \
      const unsigned d23 = pkbf((float)buf[t][2] - z4[2], (float)buf[t][3] - z4[3]); \
      const unsigned k01 = pkbf(bf_lo(d01) * s4[0], bf_hi(d01) * s4[1]); \
      const unsigned k23 = pkbf(bf_lo(d23) * s4[2], bf_hi(d23) * s4[3]); \
      u[t] = tr32(dot2(k23, uq[0][1], dot2(k01, uq[0][0], 0.f)), dot2(k23, uq[1][1], dot2(k01, uq[1][0], 0.f))); } \
    asm volatile("" : "+v"(u[0]), "+v"(u[1]), "+v"(u[2]), "+v"(u[3])); \
    PACED_LD(buf, rsn, rown, PACE); \
    const float y0 = tr16(u[0], u[2]), y1 = tr16(u[1], u[3]); \
    float sc = (b3 ? y1 : y0) + dpp<XOR8>(b3 ? y0 : y1); \
    sc += dpp<XOR1>(sc); sc += dpp<XOR2>(sc); sc += dpp<XOR7>(sc); \
    sc = bf_lo(pkbf(sc, sc)) * C2;                         \
    m = fmaxf(m, sc); \
    if ((lane & 7) == 0) S[(row_) / WAVES * 2 + swr] = sc; } while (0)
#pragma unroll 1
  for (int g = 0; g < NG; ++g) {
    if (g == 1) __syncthreads();
    if (g > 0) {
      uz = *reinterpret_cast<const u32x2*>(lds + L_A + g * DH + lane * 2);
      us = *reinterpret_cast<const u32x2*>(lds + L_A + g * DH + DH / 2 + lane * 2);
    }
    const f32x4 z4 = bf4(uz), s4 = bf4(us);
#pragma unroll 1
    for (int r = g * GRP; r < (g + 1) * GRP; r += 2 * CH) {
      const bool lastk = r + 2 * CH >= REG;
      const __amdgpu_buffer_rsrc_t rn = __builtin_amdgcn_make_buffer_rsrc((void*)(lastk ? vb : kb), 0, REG * DH * 4, 0x00020000);
      const int rnx = lastk ? 0 : r + 2 * CH;
      SCORE(bufA, r, rn, rnx);
      SCORE(bufB, r + CH, rn, rnx + CH);
    }
  }
  WAVE_SYNC();
  m = half_max(m);
  float l = 0.f, zs = 0.f;
  {
    constexpr int NI = JN / 32;
    float sc[NI]; f32x2 vsz[NI];
#pragma unroll
    for (int i = 0; i < NI; ++i) {
      const int j = i * 32 + (lane & 31);
      sc[i] = S[j * 2 + (lane >> 5)];
      vsz[i] = *reinterpret_cast<const f32x2*>(lds + L_V + 2 * (j * WAVES + wave));
    }
#pragma unroll
    for (int i = 0; i < NI; ++i) {
      const float pr = __builtin_amdgcn_exp2f(sc[i] - m);
      l += pr; zs = fmaf(pr, vsz[i][1], zs);
      S[(i * 32 + (lane & 31)) * 2 + (lane >> 5)] = pr * vsz[i][0];
    }
  }
  WAVE_SYNC();
  float acc[2][4] = {{0.f, 0.f, 0.f, 0.f}, {0.f, 0.f, 0.f, 0.f}};
#define ACCUM(buf, row_) do { \
    f32x4 pp[TT / 2]; \
    _Pragma("unroll") for (int t = 0; t < TT / 2; ++t) pp[t] = *reinterpret_cast<const f32x4*>(S + (row_) / WAVES * 2 + 4 * t); \
    _Pragma("unroll") for (int t = 0; t < TT; ++t) { \
      const float p0 = pp[t >> 1][(t & 1) * 2], p1 = pp[t >> 1][(t & 1) * 2 + 1]; \
      _Pragma("unroll") for (int e = 0; e < 4; ++e) { const float f = (float)buf[t][e]; acc[0][e] = fmaf(p0, f, acc[0][e]); acc[1][e] = fmaf(p1, f, acc[1][e]); } } \
    asm volatile("" : "+v"(acc[0][0]), "+v"(acc[0][1]), "+v"(acc[0][2]), "+v"(acc[0][3]), "+v"(acc[1][0]), "+v"(acc[1][1]), "+v"(acc[1][2]), "+v"(acc[1][3])); \
    if ((row_) + 2 * CH >= REG) tgt = 0;                   \
    PACED_LD(buf, rv, (row_) + 2 * CH, PACEV); } while (0)
#pragma unroll 1
  for (int r = 0; r < REG; r += 2 * CH) {
    ACCUM(bufA, r);
    ACCUM(bufB, r + CH);
  }

  {
    const float l_ = half_sum(l), z_ = half_sum(zs);
    const float z0 = rdlane(z_, 0), z1 = rdlane(z_, 32);
    float* rec = lds + L_R + wave * PART;
    *reinterpret_cast<f32x4*>(rec + 4 * lane) = f32x4{acc[0][0] - z0, acc[0][1] - z0, acc[0][2] - z0, acc[0][3] - z0};
    *reinterpret_cast<f32x4*>(rec + DH + 4 * lane) = f32x4{acc[1][0] - z1, acc[1][1] - z1, acc[1][2] - z1, acc[1][3] - z1};
    if ((lane & 31) == 0) { rec[2 * DH + (lane >> 5)] = m; rec[2 * DH + 2 + (lane >> 5)] = l_; }
  }
  __syncthreads();
  const int e0 = tid * 2, h = wave >> 1;
  float M = -INFINITY, L = 0.f, o0 = 0.f, o1 = 0.f;
  {
    const float* recs = lds + L_R;
    float mm[WAVES];
#pragma unroll
    for (int x = 0; x < WAVES; ++x) { mm[x] = recs[x * PART + 2 * DH + h]; M = fmaxf(M, mm[x]); }
#pragma unroll
    for (int x = 0; x < WAVES; ++x) {
      const float sw = __builtin_amdgcn_exp2f(mm[x] - M);
      const f32x2 v = *reinterpret_cast<const f32x2*>(recs + x * PART + e0);
      o0 = fmaf(sw, v[0], o0); o1 = fmaf(sw, v[1], o1);
      L = fmaf(sw, recs[x * PART + 2 * DH + 2 + h], L);
    }
  }
  u64* const mb = reinterpret_cast<u64*>(mbox + (size_t)pair * MPAIR + wave * (NSPLIT * MBOX));
  const unsigned* T = reinterpret_cast<const unsigned*>(lds + L_T);
  if (wave != slot) {
    const u32x2 t01 = *reinterpret_cast<const u32x2*>(T + (wave * 65 + lane) * 2);
    const unsigned t2 = T[(wave * 65 + 64) * 2 + (lane & 1)];
    u64* dst = mb + slot * (MBOX / 8);
    __hip_atomic_store(dst + lane, (u64)t01[0] << 32 | __float_as_uint(o0), RLX);
    __hip_atomic_store(dst + 64 + lane, (u64)t01[1] << 32 | __float_as_uint(o1), RLX);
    if (lane < 2) __hip_atomic_store(dst + 128 + lane, (u64)t2 << 32 | __float_as_uint(lane ? L : M), RLX);
  } else {
    u64 ga[NSPLIT - 1], gb[NSPLIT - 1], gc;
    u32x2 t01[NSPLIT - 1];
#pragma unroll
    for (int i = 0; i < NSPLIT - 1; ++i) t01[i] = *reinterpret_cast<const u32x2*>(T + ((4 + ((slot + 1 + i) & 3)) * 65 + lane) * 2);
    const int li = lane < 6 ? lane : 0, lj = (slot + 1 + (li >> 1)) & 3;
    const unsigned t2 = T[((4 + lj) * 65 + 64) * 2 + (li & 1)];
    const u64 t0 = __builtin_amdgcn_s_memrealtime();
    for (;;) {
#pragma unroll
      for (int i = 0; i < NSPLIT - 1; ++i) {
        const u64* src = mb + ((slot + 1 + i) & 3) * (MBOX / 8);
        ga[i] = __hip_atomic_load(src + lane, RLX); gb[i] = __hip_atomic_load(src + 64 + lane, RLX);
      }
      gc = __hip_atomic_load(mb + lj * (MBOX / 8) + 128 + (li & 1), RLX);
      bool ok = (unsigned)(gc >> 32) == t2;
#pragma unroll
      for (int i = 0; i < NSPLIT - 1; ++i) ok = ok && (unsigned)(ga[i] >> 32) == t01[i][0] && (unsigned)(gb[i] >> 32) == t01[i][1];
      if (__all(ok) || __builtin_amdgcn_s_memrealtime() - t0 > SPIN_LIMIT) break;
    }
    const float ml = __uint_as_float((unsigned)gc);
    float Mx = M;
#pragma unroll
    for (int i = 0; i < NSPLIT - 1; ++i) Mx = fmaxf(Mx, rdlane(ml, 2 * i));
    const float s0 = __builtin_amdgcn_exp2f(M - Mx);
    float a0 = s0 * o0, a1 = s0 * o1, LL = s0 * L;
#pragma unroll
    for (int i = 0; i < NSPLIT - 1; ++i) {
      const float sw = __builtin_amdgcn_exp2f(rdlane(ml, 2 * i) - Mx);
      a0 = fmaf(sw, __uint_as_float((unsigned)ga[i]), a0);
      a1 = fmaf(sw, __uint_as_float((unsigned)gb[i]), a1);
      LL = fmaf(sw, rdlane(ml, 2 * i + 1), LL);
    }
    *reinterpret_cast<unsigned*>(out + (size_t)pair * 2 * DH + e0) = pkbf(a0 / LL, a1 / LL);
  }
}

}

extern "C" void kernel_launch(void* const* d_in, const int* in_sizes, int n_in, void* d_out, int out_size, void* d_ws,
                              size_t ws_size, hipStream_t stream) {
  const unsigned short* q = (const unsigned short*)d_in[0];
  const int* qk = (const int*)d_in[1];
  const int* qv = (const int*)d_in[2];
  const unsigned short* ks = (const unsigned short*)d_in[3];
  const unsigned short* kz = (const unsigned short*)d_in[4];
  const unsigned short* vs = (const unsigned short*)d_in[5];
  const unsigned short* vz = (const unsigned short*)d_in[6];
  char* mbox = (char*)d_ws;
  u64* ctr = (u64*)((char*)d_ws + (2u << 20));
  attn_kernel<<<NWG, NTHR + 64, 0, stream>>>(q, qk, qv, ks, kz, vs, vz, mbox, ctr, (__hip_bfloat16*)d_out);
}
```

```cpp
#include <hip/hip_runtime.h>
#include <hip/hip_bf16.h>
#include <stdint.h>

namespace {

constexpr int B = 8, HQ = 16, HKV = 8, SKV = 4096, DH = 256, GRP = 128;
constexpr int NSPLIT = 4, NWG = B * HKV * NSPLIT, REG = SKV / NSPLIT;
constexpr int WAVES = 4;
constexpr int TT = 4;
constexpr int NTHR = WAVES * 64;
constexpr int CH = WAVES * TT;
constexpr int NG = REG / GRP;
constexpr int JN = REG / WAVES;
static_assert(HQ == 2 * HKV && DH == 256 && WAVES == NSPLIT && GRP % (2 * CH) == 0 && JN % 32 == 0, "shape");
constexpr unsigned PACE = 896;
constexpr unsigned PACEV = 880;
constexpr unsigned JUMP = 200;

typedef int i32x4 __attribute__((ext_vector_type(4)));
typedef float f32x4 __attribute__((ext_vector_type(4)));
typedef float f32x2 __attribute__((ext_vector_type(2)));
typedef unsigned u32x2 __attribute__((ext_vector_type(2)));
typedef unsigned u32x4 __attribute__((ext_vector_type(4)));
typedef unsigned long long u64;
typedef __bf16 bf16x2 __attribute__((ext_vector_type(2)));

__device__ __forceinline__ float bf_lo(unsigned u) { return __builtin_bit_cast(float, u << 16); }
__device__ __forceinline__ float bf_hi(unsigned u) { return __builtin_bit_cast(float, u & 0xffff0000u); }
__device__ __forceinline__ f32x4 bf4(u32x2 u) { return f32x4{bf_lo(u[0]), bf_hi(u[0]), bf_lo(u[1]), bf_hi(u[1])}; }
__device__ __forceinline__ float dot2(unsigned a, unsigned b, float c) {
  return __builtin_amdgcn_fdot2_f32_bf16(__builtin_bit_cast(bf16x2, a), __builtin_bit_cast(bf16x2, b), c, false);
}
__device__ __forceinline__ unsigned pkbf(float lo, float hi) {
  bf16x2 r; r[0] = (__bf16)lo; r[1] = (__bf16)hi;
  return __builtin_bit_cast(unsigned, r);
}

template <int CTRL> __device__ __forceinline__ float dpp(float x) {
  return __builtin_bit_cast(float, __builtin_amdgcn_mov_dpp(__builtin_bit_cast(int, x), CTRL, 0xf, 0xf, true));
}
constexpr int XOR1 = 0xB1, XOR2 = 0x4E, XOR7 = 0x141, XOR8 = 0x128;

__device__ __forceinline__ float tr32(float a, float b) {
  auto r = __builtin_amdgcn_permlane32_swap(__float_as_uint(a), __float_as_uint(b), false, false);
  return __uint_as_float(r[0]) + __uint_as_float(r[1]);
}
__device__ __forceinline__ float tr16(float a, float b) {
  auto r = __builtin_amdgcn_permlane16_swap(__float_as_uint(a), __float_as_uint(b), false, false);
  return __uint_as_float(r[0]) + __uint_as_float(r[1]);
}
__device__ __forceinline__ float rdlane(float x, int l) {
  return __builtin_bit_cast(float, __builtin_amdgcn_readlane(__builtin_bit_cast(int, x), l));
}
__device__ __forceinline__ float half_max(float x) {
  x = fmaxf(x, dpp<XOR1>(x)); x = fmaxf(x, dpp<XOR2>(x)); x = fmaxf(x, dpp<XOR7>(x)); x = fmaxf(x, dpp<XOR8>(x));
  auto r = __builtin_amdgcn_permlane16_swap(__float_as_uint(x), __float_as_uint(x), false, false);
  return fmaxf(__uint_as_float(r[0]), __uint_as_float(r[1]));
}
__device__ __forceinline__ float half_sum(float x) {
  x += dpp<XOR1>(x); x += dpp<XOR2>(x); x += dpp<XOR7>(x); x += dpp<XOR8>(x);
  return tr16(x, x);
}

constexpr int PART = 2 * DH + 4;
constexpr int L_A = 0;
constexpr int L_V = L_A + NG * DH;
constexpr int L_S = L_V + REG * 2;
constexpr int L_R = L_S + WAVES * JN * 2;
constexpr int L_T = L_R + WAVES * PART;
constexpr int L_TOTAL = L_T + 8 * 65 * 2;

constexpr int MBOX = 1152;
constexpr int MPAIR = NSPLIT * NSPLIT * MBOX;
constexpr u64 TAG = 0x5A3C96E17Bull;
#define RLX __ATOMIC_RELAXED, __HIP_MEMORY_SCOPE_AGENT
__device__ __forceinline__ void init_word(u64* w) {
  u64 v = __hip_atomic_load(w, RLX);
  if ((v >> 24) != TAG) __hip_atomic_compare_exchange_strong(w, &v, TAG << 24, __ATOMIC_RELAXED, RLX);
}
__device__ __forceinline__ u64 add_word(u64* w, u64 inc) {
  for (;;) {
    const u64 old = __hip_atomic_fetch_add(w, inc, RLX);
    if ((old >> 24) == TAG) return old;
    init_word(w);
  }
}
#define WAVE_SYNC() do { __builtin_amdgcn_fence(__ATOMIC_RELEASE, "wavefront"); __builtin_amdgcn_wave_barrier(); __builtin_amdgcn_fence(__ATOMIC_ACQUIRE, "wavefront"); } while (0)
constexpr u64 SPIN_LIMIT = 20000000ull;

__global__ void __launch_bounds__(NTHR + 64, 2) attn_kernel(const unsigned short* __restrict__ q, const int* __restrict__ qk,
                                                         const int* __restrict__ qv, const unsigned short* __restrict__ ks,
                                                         const unsigned short* __restrict__ kz, const unsigned short* __restrict__ vs,
                                                         const unsigned short* __restrict__ vz, char* mbox, u64* ctr,
                                                         __hip_bfloat16* __restrict__ out) {
  __shared__ __attribute__((aligned(16))) float lds[L_TOTAL];
  const int bid = blockIdx.x, pair = bid >> 2, slot = bid & 3;
  const int tid = threadIdx.x, lane = tid & 63, wave = __builtin_amdgcn_readfirstlane(tid >> 6);
  const unsigned short* const ksb = ks + (size_t)bid * (NG * DH);
  const unsigned short* const kzb = kz + (size_t)bid * (NG * DH);

  if (wave == WAVES) {
    {
      u32x4 a[NG / 2], z[NG / 2], s8[2], z8[2];
#pragma unroll
      for (int i = 0; i < NG / 2; ++i) {
        z[i] = __builtin_nontemporal_load(reinterpret_cast<const u32x4*>(kzb + i * 512 + lane * 8));
        a[i] = __builtin_nontemporal_load(reinterpret_cast<const u32x4*>(ksb + i * 512 + lane * 8));
      }
#pragma unroll
      for (int i = 0; i < 2; ++i) {
        s8[i] = __builtin_nontemporal_load(reinterpret_cast<const u32x4*>(vs + (size_t)bid * REG + i * 512 + lane * 8));
        z8[i] = __builtin_nontemporal_load(reinterpret_cast<const u32x4*>(vz + (size_t)bid * REG + i * 512 + lane * 8));
      }
#pragma unroll
      for (int i = 0; i < NG / 2; ++i) {
        float* t = lds + L_A + (2 * i + (lane >> 5)) * DH + (lane & 31) * 4;
        *reinterpret_cast<u32x4*>(t) = z[i]; *reinterpret_cast<u32x4*>(t + DH / 2) = a[i];
      }
#pragma unroll
      for (int i = 0; i < 2; ++i)
#pragma unroll
        for (int e = 0; e < 4; ++e) {
          const float s0 = bf_lo(s8[i][e]), s1 = bf_hi(s8[i][e]);
          *reinterpret_cast<f32x4*>(lds + L_V + (i * 512 + lane * 8 + e * 2) * 2) = f32x4{s0, s0 * bf_lo(z8[i][e]), s1, s1 * bf_hi(z8[i][e])};
        }
    }
    __syncthreads();
    u64* W = ctr + pair * 32;
    const __amdgpu_buffer_rsrc_t rp = __builtin_amdgcn_make_buffer_rsrc((void*)(mbox + (size_t)pair * MPAIR), 0, MPAIR, 0x00020000);
    unsigned ta[8], tb[8], tc[8];
#pragma unroll
    for (int e = 0; e < 8; ++e) {
      const int x = e & 3, so = x == slot ? MPAIR : (e < 4 ? (x * NSPLIT + slot) : (slot * NSPLIT + x)) * MBOX;
      ta[e] = __builtin_amdgcn_raw_buffer_load_b32(rp, lane * 8 + 4, so, 18);
      tb[e] = __builtin_amdgcn_raw_buffer_load_b32(rp, lane * 8 + 516, so, 18);
      tc[e] = __builtin_amdgcn_raw_buffer_load_b32(rp, (lane & 1) * 8 + 1028, so, 18);
    }
    unsigned* T = reinterpret_cast<unsigned*>(lds + L_T);
#pragma unroll
    for (int e = 0; e < 8; ++e) {
      *reinterpret_cast<u32x2*>(T + (e * 65 + lane) * 2) = u32x2{ta[e] + 1, tb[e] + 1};
      if (lane < 2) T[(e * 65 + 64) * 2 + lane] = tc[e] + 1;
    }
    asm volatile("s_waitcnt vmcnt(0)" ::: "memory");
    if (lane == 0) {
      init_word(W);
      u64 v = add_word(W, 1ull) + 1;
      const u64 t0 = __builtin_amdgcn_s_memrealtime();
      while ((v & 0xFFFF) != NSPLIT && __builtin_amdgcn_s_memrealtime() - t0 < SPIN_LIMIT) {
        __builtin_amdgcn_s_sleep(32);
        v = __hip_atomic_load(W, RLX);
      }
      const u64 o = __hip_atomic_fetch_add(W, 1ull << 16, RLX);
      if (((o >> 16) & 0xFF) == NSPLIT - 1) __hip_atomic_store(W, 0ull, RLX);
    }
    __syncthreads();
    return;
  }

  const int* const kb = qk + (size_t)bid * (REG * DH);
  const int* const vb = qv + (size_t)bid * (REG * DH);
  const __amdgpu_buffer_rsrc_t rk = __builtin_amdgcn_make_buffer_rsrc((void*)kb, 0, REG * DH * 4, 0x00020000);
  const __amdgpu_buffer_rsrc_t rv = __builtin_amdgcn_make_buffer_rsrc((void*)vb, 0, REG * DH * 4, 0x00020000);
  const int voff = lane * 16 + wave * 1024;
  u32x2 uq[2];
#pragma unroll
  for (int h = 0; h < 2; ++h) uq[h] = __builtin_nontemporal_load(reinterpret_cast<const u32x2*>(q + ((size_t)pair * 2 + h) * DH + 4 * lane));
  u32x2 uz = __builtin_nontemporal_load(reinterpret_cast<const u32x2*>(kzb + 4 * lane));
  u32x2 us = __builtin_nontemporal_load(reinterpret_cast<const u32x2*>(ksb + 4 * lane));
  i32x4 bufA[TT], bufB[TT];
#define LD(dst, rs, row_) do { __builtin_amdgcn_sched_barrier(0); _Pragma("unroll") for (int t_ = 0; t_ < TT; ++t_) \
    dst[t_] = __builtin_bit_cast(i32x4, __builtin_amdgcn_raw_buffer_load_b128(rs, voff, ((row_) + t_ * WAVES) * 1024, 2)); __builtin_amdgcn_sched_barrier(0); } while (0)
  LD(bufA, rk, 0); LD(bufB, rk, CH);
  asm volatile("" : "+v"(uq[0]), "+v"(uq[1]), "+v"(uz), "+v"(us), "+v"(bufB[TT - 1]));

  const unsigned tstart = (unsigned)__builtin_amdgcn_s_memrealtime();
  unsigned tgt = 0, tprev = 0;
#define PACED_LD(buf, rs, row_, pace_) do { unsigned now_; \
    while ((now_ = ((unsigned)__builtin_amdgcn_s_memrealtime() - tstart) << 4) < tgt) __builtin_amdgcn_s_sleep(1); \
    if (now_ - tprev > JUMP * 16 && now_ > 1000 * 16) tgt += now_ - tprev - (pace_); \
    tprev = now_; tgt += (pace_); \
    LD(buf, rs, row_); } while (0)

  constexpr float C2 = 0.0625f * 1.4426950408889634f;
  float* const S = lds + L_S + wave * (JN * 2);
  const int swr = ((lane >> 3) & 3) * 2 + (lane >> 5);
  const bool b3 = (lane & 8) != 0;
  float m = -INFINITY;
#define SCORE(buf, row_, rsn, rown) do { \
    float u[TT]; \
    _Pragma("unroll") for (int t = 0; t < TT; ++t) {       \
      const unsigned d01 = pkbf((float)buf[t][0] - z4[0], (float)buf[t][1] - z4[1]); \
      const unsigned d23 = pkbf((float)buf[t][2] - z4[2], (float)buf[t][3] - z4[3]); \
      const unsigned k01 = pkbf(bf_lo(d01) * s4[0], bf_hi(d01) * s4[1]); \
      const unsigned k23 = pkbf(bf_lo(d23) * s4[2], bf_hi(d23) * s4[3]); \
      u[t] = tr32(dot2(k23, uq[0][1], dot2(k01, uq[0][0], 0.f)), dot2(k23, uq[1][1], dot2(k01, uq[1][0], 0.f))); } \
    asm volatile("" : "+v"(u[0]), "+v"(u[1]), "+v"(u[2]), "+v"(u[3])); \
    PACED_LD(buf, rsn, rown, PACE); \
    const float y0 = tr16(u[0], u[2]), y1 = tr16(u[1], u[3]); \
    float sc = (b3 ? y1 : y0) + dpp<XOR8>(b3 ? y0 : y1); \
    sc += dpp<XOR1>(sc); sc += dpp<XOR2>(sc); sc += dpp<XOR7>(sc); \
    sc = bf_lo(pkbf(sc, sc)) * C2;                         \
    m = fmaxf(m, sc); \
    if ((lane & 7) == 0) S[(row_) / WAVES * 2 + swr] = sc; } while (0)
#pragma unroll 1
  for (int g = 0; g < NG; ++g) {
    if (g == 1) __syncthreads();
    if (g > 0) {
      uz = *reinterpret_cast<const u32x2*>(lds + L_A + g * DH + lane * 2);
      us = *reinterpret_cast<const u32x2*>(lds + L_A + g * DH + DH / 2 + lane * 2);
    }
    const f32x4 z4 = bf4(uz), s4 = bf4(us);
#pragma unroll 1
    for (int r = g * GRP; r < (g + 1) * GRP; r += 2 * CH) {
      const bool lastk = r + 2 * CH >= REG;
      const __amdgpu_buffer_rsrc_t rn = __builtin_amdgcn_make_buffer_rsrc((void*)(lastk ? vb : kb), 0, REG * DH * 4, 0x00020000);
      const int rnx = lastk ? 0 : r + 2 * CH;
      SCORE(bufA, r, rn, rnx);
      SCORE(bufB, r + CH, rn, rnx + CH);
    }
  }
  WAVE_SYNC();
  m = half_max(m);
  float l = 0.f, zs = 0.f;
  {
    constexpr int NI = JN / 32;
    float sc[NI]; f32x2 vsz[NI];
#pragma unroll
    for (int i = 0; i < NI; ++i) {
      const int j = i * 32 + (lane & 31);
      sc[i] = S[j * 2 + (lane >> 5)];
      vsz[i] = *reinterpret_cast<const f32x2*>(lds + L_V + 2 * (j * WAVES + wave));
    }
#pragma unroll
    for (int i = 0; i < NI; ++i) {
      const float pr = __builtin_amdgcn_exp2f(sc[i] - m);
      l += pr; zs = fmaf(pr, vsz[i][1], zs);
      S[(i * 32 + (lane & 31)) * 2 + (lane >> 5)] = pr * vsz[i][0];
    }
  }
  WAVE_SYNC();
  float acc[2][4] = {{0.f, 0.f, 0.f, 0.f}, {0.f, 0.f, 0.f, 0.f}};
#define ACCUM(buf, row_) do { \
    f32x4 pp[TT / 2]; \
    _Pragma("unroll") for (int t = 0; t < TT / 2; ++t) pp[t] = *reinterpret_cast<const f32x4*>(S + (row_) / WAVES * 2 + 4 * t); \
    _Pragma("unroll") for (int t = 0; t < TT; ++t) { \
      const float p0 = pp[t >> 1][(t & 1) * 2], p1 = pp[t >> 1][(t & 1) * 2 + 1]; \
      _Pragma("unroll") for (int e = 0; e < 4; ++e) { const float f = (float)buf[t][e]; acc[0][e] = fmaf(p0, f, acc[0][e]); acc[1][e] = fmaf(p1, f, acc[1][e]); } } \
    asm volatile("" : "+v"(acc[0][0]), "+v"(acc[0][1]), "+v"(acc[0][2]), "+v"(acc[0][3]), "+v"(acc[1][0]), "+v"(acc[1][1]), "+v"(acc[1][2]), "+v"(acc[1][3])); \
    if ((row_) + 2 * CH >= REG) tgt = 0;                   \
    PACED_LD(buf, rv, (row_) + 2 * CH, PACEV); } while (0)
#pragma unroll 1
  for (int r = 0; r < REG; r += 2 * CH) {
    ACCUM(bufA, r);
    ACCUM(bufB, r + CH);
  }

  {
    const float l_ = half_sum(l), z_ = half_sum(zs);
    const float z0 = rdlane(z_, 0), z1 = rdlane(z_, 32);
    float* rec = lds + L_R + wave * PART;
    *reinterpret_cast<f32x4*>(rec + 4 * lane) = f32x4{acc[0][0] - z0, acc[0][1] - z0, acc[0][2] - z0, acc[0][3] - z0};
    *reinterpret_cast<f32x4*>(rec + DH + 4 * lane) = f32x4{acc[1][0] - z1, acc[1][1] - z1, acc[1][2] - z1, acc[1][3] - z1};
    if ((lane & 31) == 0) { rec[2 * DH + (lane >> 5)] = m; rec[2 * DH + 2 + (lane >> 5)] = l_; }
  }
  __syncthreads();
  const int e0 = tid * 2, h = wave >> 1;
  float M = -INFINITY, L = 0.f, o0 = 0.f, o1 = 0.f;
  {
    const float* recs = lds + L_R;
    float mm[WAVES];
#pragma unroll
    for (int x = 0; x < WAVES; ++x) { mm[x] = recs[x * PART + 2 * DH + h]; M = fmaxf(M, mm[x]); }
#pragma unroll
    for (int x = 0; x < WAVES; ++x) {
      const float sw = __builtin_amdgcn_exp2f(mm[x] - M);
      const f32x2 v = *reinterpret_cast<const f32x2*>(recs + x * PART + e0);
      o0 = fmaf(sw, v[0], o0); o1 = fmaf(sw, v[1], o1);
      L = fmaf(sw, recs[x * PART + 2 * DH + 2 + h], L);
    }
  }
  u64* const mb = reinterpret_cast<u64*>(mbox + (size_t)pair * MPAIR + wave * (NSPLIT * MBOX));
  const unsigned* T = reinterpret_cast<const unsigned*>(lds + L_T);
  if (wave != slot) {
    const u32x2 t01 = *reinterpret_cast<const u32x2*>(T + (wave * 65 + lane) * 2);
    const unsigned t2 = T[(wave * 65 + 64) * 2 + (lane & 1)];
    u64* dst = mb + slot * (MBOX / 8);
    __hip_atomic_store(dst + lane, (u64)t01[0] << 32 | __float_as_uint(o0), RLX);
    __hip_atomic_store(dst + 64 + lane, (u64)t01[1] << 32 | __float_as_uint(o1), RLX);
    if (lane < 2) __hip_atomic_store(dst + 128 + lane, (u64)t2 << 32 | __float_as_uint(lane ? L : M), RLX);
  } else {
    u64 ga[NSPLIT - 1], gb[NSPLIT - 1], gc;
    u32x2 t01[NSPLIT - 1];
#pragma unroll
    for (int i = 0; i < NSPLIT - 1; ++i) t01[i] = *reinterpret_cast<const u32x2*>(T + ((4 + ((slot + 1 + i) & 3)) * 65 + lane) * 2);
    const int li = lane < 6 ? lane : 0, lj = (slot + 1 + (li >> 1)) & 3;
    const unsigned t2 = T[((4 + lj) * 65 + 64) * 2 + (li & 1)];
    const u64 t0 = __builtin_amdgcn_s_memrealtime();
    for (;;) {
#pragma unroll
      for (int i = 0; i < NSPLIT - 1; ++i) {
        const u64* src = mb + ((slot + 1 + i) & 3) * (MBOX / 8);
        ga[i] = __hip_atomic_load(src + lane, RLX); gb[i] = __hip_atomic_load(src + 64 + lane, RLX);
      }
      gc = __hip_atomic_load(mb + lj * (MBOX / 8) + 128 + (li & 1), RLX);
      bool ok = (unsigned)(gc >> 32) == t2;
#pragma unroll
      for (int i = 0; i < NSPLIT - 1; ++i) ok = ok && (unsigned)(ga[i] >> 32) == t01[i][0] && (unsigned)(gb[i] >> 32) == t01[i][1];
      if (__all(ok) || __builtin_amdgcn_s_memrealtime() - t0 > SPIN_LIMIT) break;
    }
    const float ml = __uint_as_float((unsigned)gc);
    float Mx = M;
#pragma unroll
    for (int i = 0; i < NSPLIT - 1; ++i) Mx = fmaxf(Mx, rdlane(ml, 2 * i));
    const float s0 = __builtin_amdgcn_exp2f(M - Mx);
    float a0 = s0 * o0, a1 = s0 * o1, LL = s0 * L;
#pragma unroll
    for (int i = 0; i < NSPLIT - 1; ++i) {
      const float sw = __builtin_amdgcn_exp2f(rdlane(ml, 2 * i) - Mx);
      a0 = fmaf(sw, __uint_as_float((unsigned)ga[i]), a0);
      a1 = fmaf(sw, __uint_as_float((unsigned)gb[i]), a1);
      LL = fmaf(sw, rdlane(ml, 2 * i + 1), LL);
    }
    *reinterpret_cast<unsigned*>(out + (size_t)pair * 2 * DH + e0) = pkbf(a0 / LL, a1 / LL);
  }
}

}

extern "C" void kernel_launch(void* const* d_in, const int* in_sizes, int n_in, void* d_out, int out_size, void* d_ws,
                              size_t ws_size, hipStream_t stream) {
  if (n_in != 7 || in_sizes[0] != B * HQ * DH || in_sizes[1] != B * HKV * SKV * DH || in_sizes[2] != B * HKV * SKV * DH ||
      in_sizes[3] != B * HKV * (SKV / GRP) * DH || in_sizes[4] != in_sizes[3] || in_sizes[5] != B * HKV * SKV || in_sizes[6] != in_sizes[5] ||
      out_size != B * HQ * DH || ws_size < ((size_t)4 << 20))
    return;
  const unsigned short* q = (const unsigned short*)d_in[0];
  const int* qk = (const int*)d_in[1];
  const int* qv = (const int*)d_in[2];
  const unsigned short* ks = (const unsigned short*)d_in[3];
  const unsigned short* kz = (const unsigned short*)d_in[4];
  const unsigned short* vs = (const unsigned short*)d_in[5];
  const unsigned short* vz = (const unsigned short*)d_in[6];
  char* mbox = (char*)d_ws;
  u64* ctr = (u64*)((char*)d_ws + (2u << 20));
  attn_kernel<<<NWG, NTHR + 64, 0, stream>>>(q, qk, qv, ks, kz, vs, vz, mbox, ctr, (__hip_bfloat16*)d_out);
}
```
